```python
import torch

def _softmax(x, axis=-1):
    return torch.softmax(x, dim=axis)

def _gelu(x):
    return torch.nn.functional.gelu(x, approximate='tanh')

def _silu(x):
    return torch.nn.functional.silu(x)

def _sigmoid(x):
    return torch.sigmoid(x)

def _layernorm(x, axis=-1, eps=1e-05):
    m = x.mean(dim=axis, keepdim=True)
    v = x.var(dim=axis, keepdim=True, unbiased=False)
    return (x - m) / torch.sqrt(v + eps)

def _scatter_add(tgt, idx, src, axis=0):
    return tgt.clone().index_add_(axis, idx, src)

def _segment_sum(src, seg, n):
    out = torch.zeros((n,) + src.shape[1:], dtype=src.dtype)
    return out.index_add_(0, seg, src)

def _pad(x, pw):
    flat = tuple((v for pair in reversed(pw) for v in pair))
    return torch.nn.functional.pad(x, flat)
kernel_name = 'adaLN_cond__ssm_conv_h3__glu_gated_reglu__prompt'

def setup_inputs():

    _w = lambda a: (lambda t: t.to(torch.bfloat16) if t.is_floating_point() else t)(torch.as_tensor(a))

    def _A_setup():
        B, S, D = (1, 2048, 512)
        ks = 4
        return {'q': _w((lambda *s: torch.randn(*s, dtype=torch.bfloat16) * 0.05)(B, S, D)), 'k': _w((lambda *s: torch.randn(*s, dtype=torch.bfloat16) * 0.8)(B, S, D)), 'v': _w((lambda *s: torch.randn(*s, dtype=torch.bfloat16) * 0.8)(B, S, D)), 'shift_k': _w((lambda *s: torch.randn(*s, dtype=torch.bfloat16) * 0.8)(D, ks) * 0.5), 'ssm_k': _w((lambda *s: torch.randn(*s, dtype=torch.bfloat16) * 0.8)(D, S) * (1.0 / S ** 0.5)), 'ssm_D': _w((lambda *s: torch.randn(*s, dtype=torch.bfloat16) * 0.8)(D))}
    _A_in = _A_setup()

    def _B_setup():
        B, S, D, F = (1, 2048, 512, 1792)
        return {'x': _w((lambda *s: torch.randn(*s, dtype=torch.bfloat16) * 0.05)(B, S, D)), 'W_gate': _w((lambda *s: torch.randn(*s, dtype=torch.bfloat16) * 0.8)(D, F) * (2.0 / D) ** 0.5), 'W_up': _w((lambda *s: torch.randn(*s, dtype=torch.bfloat16) * 0.8)(D, F) * (2.0 / D) ** 0.5), 'W_down': _w((lambda *s: torch.randn(*s, dtype=torch.bfloat16) * 0.8)(F, D) * (1.0 / F) ** 0.5)}
    _B_in = _B_setup()
    return {'A_q': _A_in['q'], 'A_k': _A_in['k'], 'A_v': _A_in['v'], 'A_shift_k': _A_in['shift_k'], 'A_ssm_k': _A_in['ssm_k'], 'A_ssm_D': _A_in['ssm_D'], 'B_x': _B_in['x'], 'B_W_gate': _B_in['W_gate'], 'B_W_up': _B_in['W_up'], 'B_W_down': _B_in['W_down'], 'c': _w((lambda *s: torch.randn(*s, dtype=torch.bfloat16) * 0.2)(1, 512) * 8.0), 'w_mod': _w((lambda *s: torch.randn(*s, dtype=torch.bfloat16) * 0.2)(512, 6 * 512) * 512 ** (-0.5) * 8.0)}

def reference(A_q, A_k, A_v, A_shift_k, A_ssm_k, A_ssm_D, B_x, B_W_gate, B_W_up, B_W_down, c, w_mod):

    def _A_ref(q, k, v, shift_k, ssm_k, ssm_D):
        B, S, D = q.shape
        ks = shift_k.shape[-1]
        k_pad = _pad(k, ((0, 0), (ks - 1, 0), (0, 0)))
        k_shift = torch.zeros_like(k)
        for t in range(ks):
            k_shift = k_shift + k_pad[:, t:t + S, :] * shift_k[:, ks - 1 - t][None, None, :]
        kv = k_shift * v
        L = 2 * S
        kv_pad = _pad(kv, ((0, 0), (0, S), (0, 0)))
        kf_pad = _pad(ssm_k, ((0, 0), (0, S)))
        kv_f = (lambda x, axes=(-2, -1): torch.fft.rfft2(x.float(), dim=axes))(kv_pad[:, None, :, :], axes=(1, 2))
        kf_f = (lambda x, axes=(-2, -1): torch.fft.rfft2(x.float(), dim=axes))(kf_pad[:, None, :], axes=(1, 2))
        kf_f = (lambda x, *d: x.permute(*d))(kf_f, 1, 2, 0)
        y_f = kv_f * kf_f[None, :, :, :]
        y = (lambda x, s, axes=(-2, -1): torch.fft.irfft2(x, s=s, dim=axes).to(torch.bfloat16))(y_f, (1, L), axes=(1, 2))[:, 0, :S, :]
        y = y + kv * ssm_D[None, None, :]
        return q * y

    def _B_ref(x, W_gate, W_up, W_down):
        gate = torch.einsum('bsd,df->bsf', x, W_gate)
        up = torch.einsum('bsd,df->bsf', x, W_up)
        gate = gate * (lambda x, dt: x.to(dt))(gate > 0, gate.dtype)
        h = gate * up
        return torch.einsum('bsf,fd->bsd', h, W_down)
    x = A_q
    mod = (_silu(c) @ w_mod).reshape(c.shape[0], 6, 1, -1)
    sa, ca, ga, sb, cb, gb = (mod[:, 0].clone(), mod[:, 1].clone(), mod[:, 2].clone(), mod[:, 3].clone(), mod[:, 4].clone(), mod[:, 5].clone())
    h = _layernorm(x, axis=-1) * (1 + ca) + sa
    x = x + ga * _A_ref(h, A_k, A_v, A_shift_k, A_ssm_k, A_ssm_D)
    h = _layernorm(x, axis=-1) * (1 + cb) + sb
    x = x + gb * _B_ref(h, B_W_gate, B_W_up, B_W_down)
    return x
```

```cpp
#include <hip/hip_runtime.h>
#include <hip/hip_bf16.h>
typedef unsigned short u16;
__device__ __forceinline__ float b2f(u16 x){ return __uint_as_float(((unsigned)x)<<16); }
__device__ __forceinline__ u16 f2b(float f){ unsigned u=__float_as_uint(f); u += 0x7fffu + ((u>>16)&1u); return (u16)(u>>16); }
__device__ __forceinline__ float rb(float f){ return b2f(f2b(f)); }
constexpr int S=2048, D=512, F=1792;

__global__ void k_mod(const u16* c, const u16* w, float* mod){
  int n = blockIdx.x*blockDim.x+threadIdx.x; if(n>=6*D) return;
  float acc=0; for(int k=0;k<D;k++){ float x=b2f(c[k]); float s=rb(x/(1.f+__expf(-x))); acc+=s*b2f(w[k*6*D+n]); }
  mod[n]=rb(acc);
}
__global__ void k_kv(const u16* k,const u16* v,const u16* sk,float* kv){
  int i=blockIdx.x*blockDim.x+threadIdx.x; int s=i/D,d=i%D;
  float acc=0; for(int t=0;t<4;t++){ int ss=s+t-3; float kk= ss>=0? b2f(k[ss*D+d]):0.f; acc=rb(acc+rb(kk*b2f(sk[d*4+3-t]))); }
  kv[i]=rb(acc*b2f(v[i]));
}
__global__ void k_conv(const float* kv,const u16* kf,const u16* sD,float* z){
  int i=blockIdx.x*blockDim.x+threadIdx.x; int s=i/D,d=i%D;
  float acc=0; for(int j=0;j<=s;j++) acc+=b2f(kf[d*S+j])*kv[(s-j)*D+d];
  z[i]=rb(rb(acc)+rb(kv[i]*b2f(sD[d])));
}
__device__ float bsum(float v,float* sh){ for(int o=32;o;o>>=1) v+=__shfl_xor(v,o); __syncthreads(); if((threadIdx.x&63)==0) sh[threadIdx.x>>6]=v; __syncthreads(); float r=0; for(int i=0;i<8;i++) r+=sh[i]; return r; }
__global__ void k_row(const u16* q,const float* z,const float* mod,float* x1,float* h2){
  __shared__ float sh[8]; int s=blockIdx.x,d=threadIdx.x;
  float x=b2f(q[s*D+d]);
  float m=bsum(x,sh)/D; float var=bsum((x-m)*(x-m),sh)/D; float mb=rb(m); float sd=rb(sqrtf(rb(rb(var)+1e-5f)));
  float xn=rb(rb(x-mb)/sd); float h=rb(rb(xn*rb(1.f+mod[D+d]))+mod[d]);
  float a=rb(h*z[s*D+d]); float y=rb(x+rb(mod[2*D+d]*a));
  x1[s*D+d]=y;
  m=bsum(y,sh)/D; var=bsum((y-m)*(y-m),sh)/D; mb=rb(m); sd=rb(sqrtf(rb(rb(var)+1e-5f)));
  xn=rb(rb(y-mb)/sd); h=rb(rb(xn*rb(1.f+mod[4*D+d]))+mod[3*D+d]);
  h2[s*D+d]=h;
}
__global__ void k_g1(const float* h2,const u16* wg,const u16* wu,float* hh){
  int i=blockIdx.x*blockDim.x+threadIdx.x; int s=i/F,f=i%F;
  float g=0,u=0; for(int k=0;k<D;k++){ float h=h2[s*D+k]; g+=h*b2f(wg[k*F+f]); u+=h*b2f(wu[k*F+f]); }
  g=rb(g); u=rb(u); g= g>0? g:0.f; hh[i]=rb(g*u);
}
__global__ void k_g2(const float* hh,const u16* wd,const float* x1,const float* mod,u16* out){
  int i=blockIdx.x*blockDim.x+threadIdx.x; int s=i/D,d=i%D;
  float a=0; for(int k=0;k<F;k++) a+=hh[s*F+k]*b2f(wd[k*D+d]);
  out[i]=f2b(x1[i]+rb(mod[5*D+d]*rb(a)));
}
extern "C" void kernel_launch(void* const* d_in, const int* in_sizes, int n_in, void* d_out, int out_size, void* d_ws, size_t ws_size, hipStream_t stream){
  const u16 *q=(const u16*)d_in[0],*k=(const u16*)d_in[1],*v=(const u16*)d_in[2],*sk=(const u16*)d_in[3],*kf=(const u16*)d_in[4],*sD=(const u16*)d_in[5];
  const u16 *wg=(const u16*)d_in[7],*wu=(const u16*)d_in[8],*wd=(const u16*)d_in[9],*c=(const u16*)d_in[10],*wm=(const u16*)d_in[11];
  float* ws=(float*)d_ws; float* mod=ws; float* kv=ws+4096; float* z=kv+S*D; float* x1=z+S*D; float* h2=x1+S*D; float* hh=h2+S*D;
  k_mod<<<12,256,0,stream>>>(c,wm,mod);
  k_kv<<<S*D/256,256,0,stream>>>(k,v,sk,kv);
  k_conv<<<S*D/256,256,0,stream>>>(kv,kf,sD,z);
  k_row<<<S,512,0,stream>>>(q,z,mod,x1,h2);
  k_g1<<<S*F/256,256,0,stream>>>(h2,wg,wu,hh);
  k_g2<<<S*D/256,256,0,stream>>>(hh,wd,x1,mod,(u16*)d_out);
}
```

```cpp
#include <hip/hip_runtime.h>
#include <stdint.h>

typedef unsigned short u16;
typedef unsigned int u32;
typedef __attribute__((ext_vector_type(8))) short bf16x8;
typedef __attribute__((ext_vector_type(4))) short s16x4;
typedef __attribute__((ext_vector_type(4))) float f32x4;
typedef __attribute__((ext_vector_type(2))) float f32x2;
typedef __attribute__((ext_vector_type(16))) float f32x16;
typedef __attribute__((ext_vector_type(4))) unsigned u32x4;
typedef __attribute__((ext_vector_type(2))) unsigned u32x2;
typedef __attribute__((ext_vector_type(2))) __bf16 bf2;
typedef __attribute__((address_space(3))) s16x4* lds_s4p;

#define DI __device__ __forceinline__

constexpr int S = 2048, D = 512, F = 1792;

DI float lo2f(u32 x) { return __uint_as_float(x << 16); }
DI float hi2f(u32 x) { return __uint_as_float(x & 0xffff0000u); }
DI u32 pk2(float a, float b) {
  f32x2 v = {a, b};
  return __builtin_bit_cast(u32, __builtin_convertvector(v, bf2));
}
DI float rb(float a) { return lo2f(pk2(a, a)); }
DI float dot2(u32 a, u32 b, float c) {
  return __builtin_amdgcn_fdot2_f32_bf16(__builtin_bit_cast(bf2, a), __builtin_bit_cast(bf2, b), c, false);
}

constexpr size_t WS_MOD = 0;
constexpr size_t WS_OPC = WS_MOD + 3072 * 4;
constexpr size_t WS_SBB = WS_OPC + 1024 * 4;
constexpr size_t WS_STATS = 32768;
constexpr size_t WS_URT = 65536;
constexpr size_t WS_QT2 = WS_URT + 2u * S * D;
constexpr size_t WS_X1F = WS_QT2 + 2u * S * D;
constexpr size_t WS_W1F = WS_X1F + 2u * S * D;
constexpr size_t W1F_CB = 33 * 7 * 1024;
constexpr size_t WS_WDF = WS_W1F + 16 * W1F_CB;
constexpr size_t WS_HHF = WS_WDF + 2u * F * D;
constexpr size_t WS_END = WS_HHF + 2u * S * F;

constexpr int K0_NA = 256, K0_NB = 128, K0_NC = 96;

__global__ __launch_bounds__(256) void k0_prep(const u16* __restrict__ q, const u16* __restrict__ k,
                                               const u16* __restrict__ v, const u16* __restrict__ shk,
                                               const u16* __restrict__ cc, const u16* __restrict__ wmod,
                                               char* __restrict__ ws) {
  __shared__ __attribute__((aligned(16))) u32 sm[4096];
  const int b = blockIdx.x, t = threadIdx.x;
  if (b < K0_NA) {
    const int sb = b >> 3, db = b & 7;
    const int rp = t >> 3, ci = t & 7;
    const int s0 = 64 * sb + 2 * rp, ch0 = 64 * db + 8 * ci;
    u32x4 kr[5], vr[2], qr[2], wr[4];
#pragma unroll
    for (int i = 0; i < 5; ++i) {
      int s = s0 - 3 + i;
      u32x4 z = {0, 0, 0, 0};
      kr[i] = (s >= 0) ? *(const u32x4*)(k + (size_t)s * D + ch0) : z;
    }
#pragma unroll
    for (int i = 0; i < 2; ++i) {
      vr[i] = *(const u32x4*)(v + (size_t)(s0 + i) * D + ch0);
      qr[i] = *(const u32x4*)(q + (size_t)(s0 + i) * D + ch0);
    }
#pragma unroll
    for (int i = 0; i < 4; ++i) wr[i] = *(const u32x4*)(shk + (size_t)ch0 * 4 + i * 8);
    u32* Tu = sm;
    u32* Tq = sm + 2048;
    const int x = 31 - rp;
#pragma unroll
    for (int j = 0; j < 8; ++j) {
      u32 w01 = wr[j >> 1][(j & 1) * 2], w23 = wr[j >> 1][(j & 1) * 2 + 1];
      float w0 = lo2f(w01), w1 = hi2f(w01), w2 = lo2f(w23), w3 = hi2f(w23);
      float kk[5];
#pragma unroll
      for (int i = 0; i < 5; ++i) kk[i] = (j & 1) ? hi2f(kr[i][j >> 1]) : lo2f(kr[i][j >> 1]);
      u32 p = pk2(kk[0] * w3, kk[1] * w3);
      float a0 = lo2f(p), a1 = hi2f(p);
      p = pk2(kk[1] * w2, kk[2] * w2);
      p = pk2(a0 + lo2f(p), a1 + hi2f(p));
      a0 = lo2f(p); a1 = hi2f(p);
      p = pk2(kk[2] * w1, kk[3] * w1);
      p = pk2(a0 + lo2f(p), a1 + hi2f(p));
      a0 = lo2f(p); a1 = hi2f(p);
      p = pk2(kk[3] * w0, kk[4] * w0);
      p = pk2(a0 + lo2f(p), a1 + hi2f(p));
      a0 = lo2f(p); a1 = hi2f(p);
      float v0 = (j & 1) ? hi2f(vr[0][j >> 1]) : lo2f(vr[0][j >> 1]);
      float v1 = (j & 1) ? hi2f(vr[1][j >> 1]) : lo2f(vr[1][j >> 1]);
      u32 u = pk2(a1 * v1, a0 * v0);
      int ch = 8 * ci + j;
      Tu[ch * 32 + ((((x >> 2) ^ ci) << 2) | (x & 3))] = u;
    }
#pragma unroll
    for (int j = 0; j < 4; ++j) {
      int pr = 4 * ci + j;
      int xs = 2 * rp;
      u32x2 val = {qr[0][j], qr[1][j]};
      *(u32x2*)&Tq[pr * 64 + ((((xs >> 2) ^ (2 * ci)) << 2) | (xs & 3))] = val;
    }
    __syncthreads();
    u16* urT = (u16*)(ws + WS_URT);
    u32* qT2 = (u32*)(ws + WS_QT2);
    const int tbase = S - 64 * (sb + 1);
#pragma unroll
    for (int i = 0; i < 2; ++i) {
      int idx = t + 256 * i;
      int ch = idx >> 3, c8 = idx & 7;
      u32x4 val = *(const u32x4*)&Tu[ch * 32 + ((c8 ^ (ch >> 3)) << 2)];
      *(u32x4*)(urT + (size_t)(64 * db + ch) * S + tbase + c8 * 8) = val;
      int pr = idx >> 4, c16 = idx & 15;
      u32x4 qv = *(const u32x4*)&Tq[pr * 64 + ((c16 ^ (2 * (pr >> 2))) << 2)];
      *(u32x4*)(qT2 + (size_t)(32 * db + pr) * S + 64 * sb + c16 * 4) = qv;
    }
  } else if (b < K0_NA + K0_NB) {
    const int w = t >> 6, lane = t & 63;
    const int r0 = (b - K0_NA) * 16 + w * 4;
    u32x4 xr[4];
#pragma unroll
    for (int i = 0; i < 4; ++i) xr[i] = *(const u32x4*)(q + (size_t)(r0 + i) * D + lane * 8);
    float sum[4];
#pragma unroll
    for (int i = 0; i < 4; ++i) {
      float a = 0.f;
#pragma unroll
      for (int j = 0; j < 4; ++j) a += lo2f(xr[i][j]) + hi2f(xr[i][j]);
      sum[i] = a;
    }
#pragma unroll
    for (int o = 32; o; o >>= 1)
#pragma unroll
      for (int i = 0; i < 4; ++i) sum[i] += __shfl_xor(sum[i], o);
    float sq[4];
#pragma unroll
    for (int i = 0; i < 4; ++i) {
      float m = sum[i] * (1.f / D), a = 0.f;
#pragma unroll
      for (int j = 0; j < 4; ++j) {
        float d0 = lo2f(xr[i][j]) - m, d1 = hi2f(xr[i][j]) - m;
        a += d0 * d0 + d1 * d1;
      }
      sq[i] = a;
    }
#pragma unroll
    for (int o = 32; o; o >>= 1)
#pragma unroll
      for (int i = 0; i < 4; ++i) sq[i] += __shfl_xor(sq[i], o);
    if (lane < 4) {
      float sm_ = lane == 0 ? sum[0] : lane == 1 ? sum[1] : lane == 2 ? sum[2] : sum[3];
      float sq_ = lane == 0 ? sq[0] : lane == 1 ? sq[1] : lane == 2 ? sq[2] : sq[3];
      float mb = rb(sm_ * (1.f / D));
      float sd = rb(sqrtf(rb(rb(sq_ * (1.f / D)) + 1e-5f)));
      f32x2 o = {mb, 1.0f / sd};
      ((f32x2*)(ws + WS_STATS))[r0 + lane] = o;
    }
  } else {
    const int cb = b - K0_NA - K0_NB;
    const int kq = t >> 2, cq = t & 3;
    u32x4 wv[8];
#pragma unroll
    for (int i = 0; i < 8; ++i) wv[i] = *(const u32x4*)(wmod + (size_t)(8 * kq + i) * (6 * D) + 32 * cb + 8 * cq);
    u32x4 cv = *(const u32x4*)(cc + 8 * kq);
    float acc[8];
#pragma unroll
    for (int j = 0; j < 8; ++j) acc[j] = 0.f;
#pragma unroll
    for (int i = 0; i < 8; ++i) {
      float x = (i & 1) ? hi2f(cv[i >> 1]) : lo2f(cv[i >> 1]);
      float sl = rb(x / (1.f + expf(-x)));
#pragma unroll
      for (int j = 0; j < 4; ++j) {
        acc[2 * j] += sl * lo2f(wv[i][j]);
        acc[2 * j + 1] += sl * hi2f(wv[i][j]);
      }
    }
#pragma unroll
    for (int o = 4; o < 64; o <<= 1)
#pragma unroll
      for (int j = 0; j < 8; ++j) acc[j] += __shfl_xor(acc[j], o);
    float* red = (float*)sm;
    const int w = t >> 6, lane = t & 63;
    if (lane < 4) {
#pragma unroll
      for (int j = 0; j < 8; ++j) red[w * 32 + lane * 8 + j] = acc[j];
    }
    __syncthreads();
    if (t < 32) {
      float r = rb(red[t] + red[32 + t] + red[64 + t] + red[96 + t]);
      int n = 32 * cb + t;
      ((float*)(ws + WS_MOD))[n] = r;
      int seg = n >> 9, d = n & 511;
      if (seg == 1) ((float*)(ws + WS_OPC))[d] = rb(1.f + r);
      if (seg == 4) ((float*)(ws + WS_OPC))[512 + d] = rb(1.f + r);
      if (seg == 3) ((u16*)(ws + WS_SBB))[d] = (u16)(__float_as_uint(r) >> 16);
    }
  }
}

constexpr int K1_CS = 4160;
constexpr int K1_FC = 0;
constexpr int K1_U = 66560;
constexpr int K1_PART = K1_U + 2 * 4608;
constexpr int K1_WST = K1_PART + 36864;
constexpr int K1_RED = K1_WST + 24576;
constexpr int K1_LDS = K1_RED + 1024;

__global__ __launch_bounds__(512, 2) void k1_conv(const u16* __restrict__ kf, const u16* __restrict__ ssmD,
                                                  const u16* __restrict__ wg, const u16* __restrict__ wu,
                                                  const u16* __restrict__ wd, char* __restrict__ ws) {
  extern __shared__ __attribute__((aligned(16))) char sm[];
  const int b = blockIdx.x, t = threadIdx.x;
  const int p = (b & 7) * 32 + (b >> 3);
  const int w = __builtin_amdgcn_readfirstlane(t >> 6), lane = t & 63;
  const float* mod = (const float*)(ws + WS_MOD);
  const float* opc = (const float*)(ws + WS_OPC);

  const int ch = t >> 8, ci = t & 255;
  const u16* kfr = kf + (size_t)(2 * p + ch) * S;
  u32x4 ka = {0, 0, 0, 0}, kb;
  if (ci > 0) ka = *(const u32x4*)(kfr + (ci - 1) * 8);
  kb = *(const u32x4*)(kfr + ci * 8);
  u32x4 uu = *(const u32x4*)((const u16*)(ws + WS_URT) + (size_t)(2 * p + ch) * S + ci * 8);
  u32 qp[4];
  f32x2 st[4];
#pragma unroll
  for (int i = 0; i < 4; ++i) {
    qp[i] = ((const u32*)(ws + WS_QT2))[(size_t)p * S + t + 512 * i];
    st[i] = ((const f32x2*)(ws + WS_STATS))[t + 512 * i];
  }
  const int wp = w - 4;
  const int mtg = b >> 1, kh = b & 1;
  u32x4 wl[6];
  if (w >= 4) {
    const int row = lane >> 2, part = lane & 3;
    if (b < 224) {
      const u16* src = (part & 2) ? wu : wg;
#pragma unroll
      for (int i = 0; i < 4; ++i) {
        int kk = 16 * (16 * kh + 4 * wp + i) + row;
        wl[i] = *(const u32x4*)(src + (size_t)kk * F + 16 * mtg + 8 * (part & 1));
      }
    }
    const int nt = b & 15, ks0 = 7 * (b >> 4);
#pragma unroll
    for (int i = 0; i < 2; ++i) {
      int fi = wp + 4 * i;
      if (fi < 7) wl[4 + i] = *(const u32x4*)(wd + (size_t)(16 * (ks0 + fi) + row) * D + 32 * nt + 8 * part);
    }
  }

  {
    u32 wv[8] = {ka[0], ka[1], ka[2], ka[3], kb[0], kb[1], kb[2], kb[3]};
    u32 od[7];
#pragma unroll
    for (int i = 0; i < 7; ++i) od[i] = __builtin_amdgcn_alignbit(wv[i + 1], wv[i], 16);
    char* base = sm + K1_FC + ch * (8 * K1_CS) + (ci + 1) * 16;
#pragma unroll
    for (int r = 0; r < 8; ++r) {
      const int sg = r + 1;
      u32x4 val;
      if (sg & 1) {
        const int k0 = (sg - 1) / 2;
        val = (u32x4){od[k0], od[k0 + 1], od[k0 + 2], od[k0 + 3]};
      } else {
        const int k0 = sg / 2;
        val = (u32x4){wv[k0], wv[k0 + 1], wv[k0 + 2], wv[k0 + 3]};
      }
      *(u32x4*)(base + r * K1_CS) = val;
    }
    u32x4 z = {0, 0, 0, 0};
    if (ci < 8) *(u32x4*)(sm + K1_FC + ch * (8 * K1_CS) + ci * K1_CS) = z;
    *(u32x4*)(sm + K1_U + ch * 4608 + ci * 16) = uu;
    if (ci < 32) *(u32x4*)(sm + K1_U + ch * 4608 + 4096 + ci * 16) = z;
  }
  const float sa0 = mod[2 * p], sa1 = mod[2 * p + 1];
  const float oc0 = opc[2 * p], oc1 = opc[2 * p + 1];
  const float ga0 = mod[1024 + 2 * p], ga1 = mod[1024 + 2 * p + 1];
  const u32 dpair = ((const u32*)ssmD)[p];
  u32 h1[4];
#pragma unroll
  for (int i = 0; i < 4; ++i) {
    u32 x = pk2(lo2f(qp[i]) - st[i][0], hi2f(qp[i]) - st[i][0]);
    x = pk2(lo2f(x) * st[i][1], hi2f(x) * st[i][1]);
    x = pk2(lo2f(x) * oc0, hi2f(x) * oc1);
    h1[i] = pk2(lo2f(x) + sa0, hi2f(x) + sa1);
  }
  __syncthreads();

  if (w < 4) {
    const int cch = w >> 1, c0 = 4 * (w & 1);
    const int l15 = lane & 15, h = lane >> 4;
    const char* fb = sm + K1_FC + cch * (8 * K1_CS) + (l15 & 7) * K1_CS + 2 * ((l15 & 8) + 8 * h) + 64 * c0;
    const char* gb = sm + K1_U + cch * 4608 + 2 * (2032 - 256 * 7 - 16 * l15 + 8 * h) + 64 * c0;
    f32x4 acc[8];
#pragma unroll
    for (int m = 0; m < 8; ++m) acc[m] = (f32x4){0.f, 0.f, 0.f, 0.f};
#pragma unroll
    for (int c = 0; c < 4; ++c) {
      bf16x8 Fr[8], Gr[8];
#pragma unroll
      for (int n = 0; n < 8; ++n) {
        Fr[n] = *(const bf16x8*)(fb + 512 * n + 64 * c);
        Gr[n] = *(const bf16x8*)(gb + 512 * (7 - n) + 64 * c);
      }
#pragma unroll
      for (int m = 0; m < 8; ++m)
#pragma unroll
        for (int n = 0; n <= m; ++n)
          acc[m] = __builtin_amdgcn_mfma_f32_16x16x32_bf16(Fr[n], Gr[m - n], acc[m], 0, 0, 0);
    }
    float* part = (float*)(sm + K1_PART) + w * (8 * 288) + h * 72 + l15 * 4;
#pragma unroll
    for (int m = 0; m < 8; ++m) *(f32x4*)(part + m * 288) = acc[m];
  } else {
    char* wst = sm + K1_WST + wp * 6144;
    const int g = lane >> 4, h = lane >> 5, qq = (lane & 15) >> 2, pp = lane & 3;
    if (b < 224) {
#pragma unroll
      for (int i = 0; i < 4; ++i) *(u32x4*)(wst + i * 1024 + lane * 16) = wl[i];
    }
#pragma unroll
    for (int i = 0; i < 2; ++i)
      if (wp + 4 * i < 7) *(u32x4*)(wst + (4 + i) * 1024 + lane * 16) = wl[4 + i];
    __builtin_amdgcn_fence(__ATOMIC_RELEASE, "wavefront");
    __builtin_amdgcn_wave_barrier();
    __builtin_amdgcn_fence(__ATOMIC_ACQUIRE, "wavefront");
    if (b < 224) {
      float cs = 0.f, bi = 0.f;
      char* dst = ws + WS_W1F + (size_t)(mtg / 7) * W1F_CB + (mtg % 7) * 1024 + lane * 16;
#pragma unroll
      for (int i = 0; i < 4; ++i) {
        const int ks = 16 * kh + 4 * wp + i;
        const char* a0 = wst + i * 1024 + (8 * h + qq) * 64 + (g & 1) * 32 + pp * 8;
        s16x4 lo = __builtin_amdgcn_ds_read_tr16_b64_v4i16((lds_s4p)(a0));
        s16x4 hi = __builtin_amdgcn_ds_read_tr16_b64_v4i16((lds_s4p)(a0 + 256));
        u32x2 l2 = __builtin_bit_cast(u32x2, lo), h2 = __builtin_bit_cast(u32x2, hi);
        u32 wv[4] = {l2[0], l2[1], h2[0], h2[1]};
        const f32x4 sc0 = *(const f32x4*)(opc + 512 + 16 * ks + 8 * h);
        const f32x4 sc1 = *(const f32x4*)(opc + 512 + 16 * ks + 8 * h + 4);
        const u32x4 sbv = *(const u32x4*)((const u16*)(ws + WS_SBB) + 16 * ks + 8 * h);
        u32x4 o;
        o[0] = pk2(lo2f(wv[0]) * sc0[0], hi2f(wv[0]) * sc0[1]);
        o[1] = pk2(lo2f(wv[1]) * sc0[2], hi2f(wv[1]) * sc0[3]);
        o[2] = pk2(lo2f(wv[2]) * sc1[0], hi2f(wv[2]) * sc1[1]);
        o[3] = pk2(lo2f(wv[3]) * sc1[2], hi2f(wv[3]) * sc1[3]);
#pragma unroll
        for (int j = 0; j < 4; ++j) {
          cs = dot2(o[j], 0x3f803f80u, cs);
          bi = dot2(wv[j], sbv[j], bi);
        }
        *(u32x4*)(dst + (size_t)ks * 7168) = o;
      }
      cs += __shfl_xor(cs, 32);
      bi += __shfl_xor(bi, 32);
      if (lane < 32) {
        f32x2 r = {cs, bi};
        *(f32x2*)(sm + K1_RED + (wp * 32 + lane) * 8) = r;
      }
    }
    {
      const int nt = b & 15, ks0 = 7 * (b >> 4);
#pragma unroll
      for (int i = 0; i < 2; ++i) {
        const int fi = wp + 4 * i;
        if (fi < 7) {
          const char* a0 = wst + (4 + i) * 1024 + (4 * h + qq) * 64 + (g & 1) * 32 + pp * 8;
          s16x4 lo = __builtin_amdgcn_ds_read_tr16_b64_v4i16((lds_s4p)(a0));
          s16x4 hi = __builtin_amdgcn_ds_read_tr16_b64_v4i16((lds_s4p)(a0 + 512));
          u32x2 l2 = __builtin_bit_cast(u32x2, lo), h2 = __builtin_bit_cast(u32x2, hi);
          u32x4 o = {l2[0], l2[1], h2[0], h2[1]};
          *(u32x4*)(ws + WS_WDF + ((size_t)nt * 112 + ks0 + fi) * 1024 + lane * 16) = o;
        }
      }
    }
  }
  __syncthreads();

  {
    const float D0 = lo2f(dpair), D1 = hi2f(dpair);
    const float* part = (const float*)(sm + K1_PART);
    const u16* U0 = (const u16*)(sm + K1_U);
    const u16* U1 = (const u16*)(sm + K1_U + 4608);
    const int d0 = 2 * p;
    char* dst = ws + WS_X1F + ((size_t)(d0 >> 4) * 64 + 32 * ((d0 >> 3) & 1)) * 16 + 2 * (d0 & 7);
#pragma unroll
    for (int i = 0; i < 4; ++i) {
      const int s = t + 512 * i;
      const int off = (s >> 8) * 288 + ((s >> 2) & 3) * 72 + ((s >> 4) & 15) * 4 + (s & 3);
      float y0 = part[off] + part[2304 + off];
      float y1 = part[2 * 2304 + off] + part[3 * 2304 + off];
      float u0 = lo2f(U0[2047 - s]), u1 = lo2f(U1[2047 - s]);
      u32 y = pk2(y0, y1);
      u32 ud = pk2(u0 * D0, u1 * D1);
      u32 z = pk2(lo2f(y) + lo2f(ud), hi2f(y) + hi2f(ud));
      u32 a = pk2(lo2f(h1[i]) * lo2f(z), hi2f(h1[i]) * hi2f(z));
      a = pk2(ga0 * lo2f(a), ga1 * hi2f(a));
      u32 x1 = pk2(lo2f(qp[i]) + lo2f(a), hi2f(qp[i]) + hi2f(a));
      *(u32*)(dst + (size_t)(s >> 5) * 32768 + (s & 31) * 16) = x1;
    }
    if (b < 224 && t < 32) {
      const f32x2* red = (const f32x2*)(sm + K1_RED);
      f32x2 r = red[t] + red[32 + t] + red[64 + t] + red[96 + t];
      float c0 = rb(r[0]), c1 = rb(r[0] - c0), c2 = rb(r[0] - c0 - c1);
      float b0 = rb(r[1]), b1 = rb(r[1] - b0), b2 = rb(r[1] - b0 - b1);
      u32x4 o = {pk2(c0, c1), pk2(c2, b0), pk2(b1, b2), 0u};
      *(u32x4*)(ws + WS_W1F + (size_t)(mtg / 7) * W1F_CB + 32 * 7168 + (mtg % 7) * 1024 + (t + 32 * kh) * 16) = o;
    }
  }
}

constexpr int K2_G = 28672;
constexpr int K2_NS = 5;
constexpr int K2_COR = K2_NS * K2_G;
constexpr int K2_LDS = K2_COR + 8192;

DI void glds16(const void* g, void* l) {
  __builtin_amdgcn_global_load_lds((const __attribute__((address_space(1))) void*)g,
                                   (__attribute__((address_space(3))) void*)l, 16, 0, 0);
}
#define GLOAD(dst, ptr, off) asm volatile("global_load_dwordx4 %0, %1, off offset:" #off : "=v"(dst) : "v"(ptr) : "memory")

__global__ __launch_bounds__(256, 1) void k2_gemm1(char* __restrict__ ws) {
  extern __shared__ __attribute__((aligned(16))) char sm[];
  const int b = blockIdx.x, t = threadIdx.x;
  const int w = __builtin_amdgcn_readfirstlane(t >> 6), lane = t & 63;
  const int xcd = b & 7, idx = b >> 3;
  const int rbk = (xcd & 1) * 8 + (idx & 7);
  const int cbk = (xcd >> 1) * 4 + (idx >> 3);
  const int st = rbk * 4 + w;
  const char* wsrc = ws + WS_W1F + (size_t)cbk * W1F_CB + w * 1024 + lane * 16;
  const char* asrc = ws + WS_X1F + (size_t)st * 32768 + lane * 16;
  char* ldst = sm + w * 1024;

  u32x4 act[32];
#define ACT4(g)                                              \
  {                                                          \
    const char* ap = asrc + (g) * 4096;                      \
    GLOAD(act[4 * (g) + 0], ap, 0);                          \
    GLOAD(act[4 * (g) + 1], ap, 1024);                       \
    GLOAD(act[4 * (g) + 2], ap, 2048);                       \
    GLOAD(act[4 * (g) + 3], ap, 3072);                       \
  }
#define DMA7(g)                                                                          \
  {                                                                                      \
    _Pragma("unroll") for (int i = 0; i < 7; ++i)                                        \
        glds16(wsrc + (size_t)(g) * K2_G + i * 4096, ldst + ((g) % K2_NS) * K2_G + i * 4096); \
  }
  ACT4(0) DMA7(0) ACT4(1) DMA7(1) ACT4(2) DMA7(2) ACT4(3) DMA7(3) ACT4(4) DMA7(4)
  ACT4(5) ACT4(6) ACT4(7)
  glds16(wsrc + 8 * K2_G, ldst + K2_COR);
  glds16(wsrc + 8 * K2_G + (w == 3 ? 3072 : 4096), ldst + K2_COR + 4096);

  f32x16 acc[7];
#pragma unroll
  for (int m = 0; m < 7; ++m)
#pragma unroll
    for (int r = 0; r < 16; ++r) acc[m][r] = 0.f;
  float sum = 0.f, sq = 0.f;
  const char* lsrc = sm + lane * 16;

#define WAITG(g, n)                                                                                           \
  asm volatile("s_waitcnt vmcnt(" #n ")"                                                                      \
               : "+v"(act[4 * (g)]), "+v"(act[4 * (g) + 1]), "+v"(act[4 * (g) + 2]), "+v"(act[4 * (g) + 3]) \
               :                                                                                              \
               : "memory");                                                                                   \
  __builtin_amdgcn_s_barrier();                                                                               \
  asm volatile("" ::: "memory");
#define COMP(g)                                                                                       \
  {                                                                                                   \
    _Pragma("unroll") for (int kk = 0; kk < 4; ++kk) {                                                \
      const u32x4 a = act[4 * (g) + kk];                                                              \
      _Pragma("unroll") for (int m = 0; m < 7; ++m) {                                                 \
        bf16x8 wf = *(const bf16x8*)(lsrc + ((g) % K2_NS) * K2_G + kk * 7168 + m * 1024);             \
        acc[m] = __builtin_amdgcn_mfma_f32_32x32x16_bf16(wf, __builtin_bit_cast(bf16x8, a), acc[m], 0, 0, 0); \
      }                                                                                               \
      _Pragma("unroll") for (int j = 0; j < 4; ++j) {                                                 \
        sum = dot2(a[j], 0x3f803f80u, sum);                                                           \
        sq = dot2(a[j], a[j], sq);                                                                    \
      }                                                                                               \
    }                                                                                                 \
    asm volatile("s_waitcnt lgkmcnt(0)" ::: "memory");                                                \
  }
  WAITG(0, 58) COMP(0)
  WAITG(1, 47) DMA7(5) COMP(1)
  WAITG(2, 43) DMA7(6) COMP(2)
  WAITG(3, 39) DMA7(7) COMP(3)
  WAITG(4, 35) COMP(4)
  WAITG(5, 14) COMP(5)
  WAITG(6, 7) COMP(6)
  WAITG(7, 0) COMP(7)

  sum += __shfl_xor(sum, 32);
  sq += __shfl_xor(sq, 32);
  const float mean = sum * (1.f / D);
  const float var = fmaxf(sq * (1.f / D) - mean * mean, 0.f);
  const float mb = rb(mean);
  const float sd = rb(sqrtf(rb(rb(var) + 1e-5f)));
  const float rs = 1.0f / sd;
  const float rs2 = rs * rs;
  u32x4 cb4 = {pk2(-mb, -mb), pk2(-mb, sd), pk2(sd, sd), 0u};
  char* hdst = ws + WS_HHF + ((size_t)st * 112 + cbk * 7) * 1024 + lane * 16;
#pragma unroll
  for (int m = 0; m < 7; ++m) {
    bf16x8 wf = *(const bf16x8*)(lsrc + K2_COR + m * 1024);
    acc[m] = __builtin_amdgcn_mfma_f32_32x32x16_bf16(wf, __builtin_bit_cast(bf16x8, cb4), acc[m], 0, 0, 0);
    u32x4 o;
#pragma unroll
    for (int j = 0; j < 4; ++j) {
      float h0 = fmaxf(acc[m][2 * j], 0.f) * acc[m][8 + 2 * j] * rs2;
      float h1 = fmaxf(acc[m][2 * j + 1], 0.f) * acc[m][8 + 2 * j + 1] * rs2;
      o[j] = pk2(h0, h1);
    }
    *(u32x4*)(hdst + m * 1024) = o;
  }
}

constexpr int K3_LDS = 4 * 4 * 4096;

__global__ __launch_bounds__(256, 1) void k3_gemm2(const char* __restrict__ ws, u16* __restrict__ out) {
  extern __shared__ __attribute__((aligned(16))) char sm[];
  const int b = blockIdx.x, t = threadIdx.x;
  const int w = __builtin_amdgcn_readfirstlane(t >> 6), lane = t & 63;
  const int xcd = b & 7, idx = b >> 3;
  const int sbk = xcd * 4 + (idx & 3);
  const int nbk = idx >> 2;
  const char* hs = ws + WS_HHF + ((size_t)(2 * sbk) * 112 + 28 * w) * 1024 + lane * 16;
  const char* wsrc = ws + WS_WDF + ((size_t)(2 * nbk) * 112 + 28 * w) * 1024 + lane * 16;
  constexpr int WIN = 14;
  bf16x8 hb[WIN][2], wb[WIN][2];
#pragma unroll
  for (int i = 0; i < WIN; ++i) {
    wb[i][0] = *(const bf16x8*)(wsrc + i * 1024);
    hb[i][0] = *(const bf16x8*)(hs + i * 1024);
    wb[i][1] = *(const bf16x8*)(wsrc + 112 * 1024 + i * 1024);
    hb[i][1] = *(const bf16x8*)(hs + 112 * 1024 + i * 1024);
  }
  const int fn = w >> 1, fs = w & 1;
  const int h = lane >> 5;
  const int ncol = 64 * nbk + 32 * fn;
  const char* xsrc = ws + WS_X1F + ((size_t)(2 * sbk + fs) * 32 + (ncol >> 4)) * 1024 + lane * 16;
  u32x4 xr[2];
  xr[0] = *(const u32x4*)(xsrc);
  xr[1] = *(const u32x4*)(xsrc + 1024);
  const float* gbp = (const float*)(ws + WS_MOD) + 5 * D + ncol + 8 * h;
  f32x4 gv[4];
  gv[0] = *(const f32x4*)(gbp);
  gv[1] = *(const f32x4*)(gbp + 4);
  gv[2] = *(const f32x4*)(gbp + 16);
  gv[3] = *(const f32x4*)(gbp + 20);

  f32x16 acc[2][2];
#pragma unroll
  for (int a = 0; a < 2; ++a)
#pragma unroll
    for (int c = 0; c < 2; ++c)
#pragma unroll
      for (int r = 0; r < 16; ++r) acc[a][c][r] = 0.f;
#pragma unroll
  for (int i = 0; i < 28; ++i) {
    const int j = i % WIN;
#pragma unroll
    for (int a = 0; a < 2; ++a)
#pragma unroll
      for (int c = 0; c < 2; ++c)
        acc[a][c] = __builtin_amdgcn_mfma_f32_32x32x16_bf16(wb[j][a], hb[j][c], acc[a][c], 0, 0, 0);
    if (i + WIN < 28) {
      wb[j][0] = *(const bf16x8*)(wsrc + (i + WIN) * 1024);
      hb[j][0] = *(const bf16x8*)(hs + (i + WIN) * 1024);
      wb[j][1] = *(const bf16x8*)(wsrc + 112 * 1024 + (i + WIN) * 1024);
      hb[j][1] = *(const bf16x8*)(hs + 112 * 1024 + (i + WIN) * 1024);
    }
  }
#pragma unroll
  for (int a = 0; a < 2; ++a)
#pragma unroll
    for (int c = 0; c < 2; ++c) {
      const int tile = a * 2 + c;
      if (tile != w) {
        char* dst = sm + (w * 4 + tile) * 4096 + lane * 16;
#pragma unroll
        for (int r = 0; r < 4; ++r)
          *(f32x4*)(dst + r * 1024) =
              (f32x4){acc[a][c][4 * r], acc[a][c][4 * r + 1], acc[a][c][4 * r + 2], acc[a][c][4 * r + 3]};
      }
    }
  __syncthreads();
  f32x16 fin;
  if (w == 0) fin = acc[0][0];
  else if (w == 1) fin = acc[0][1];
  else if (w == 2) fin = acc[1][0];
  else fin = acc[1][1];
#pragma unroll
  for (int o = 1; o < 4; ++o) {
    const int ow = (w + o) & 3;
    const char* src = sm + (ow * 4 + w) * 4096 + lane * 16;
#pragma unroll
    for (int r = 0; r < 4; ++r) {
      f32x4 v = *(const f32x4*)(src + r * 1024);
#pragma unroll
      for (int e = 0; e < 4; ++e) fin[4 * r + e] += v[e];
    }
  }
  u32 pkd[8];
#pragma unroll
  for (int j = 0; j < 8; ++j) pkd[j] = pk2(fin[2 * j], fin[2 * j + 1]);
  const int srow = 64 * sbk + 32 * fs + (lane & 31);
  u16* orow = out + (size_t)srow * D + ncol + 8 * h;
#pragma unroll
  for (int kk = 0; kk < 2; ++kk) {
    auto r0 = __builtin_amdgcn_permlane32_swap(pkd[4 * kk], pkd[4 * kk + 2], false, false);
    auto r1 = __builtin_amdgcn_permlane32_swap(pkd[4 * kk + 1], pkd[4 * kk + 3], false, false);
    u32 m4[4] = {r0[0], r1[0], r0[1], r1[1]};
    u32x4 o;
#pragma unroll
    for (int j = 0; j < 4; ++j) {
      float g0 = gv[2 * kk + (j >> 1)][(j & 1) * 2], g1 = gv[2 * kk + (j >> 1)][(j & 1) * 2 + 1];
      u32 a = pk2(g0 * lo2f(m4[j]), g1 * hi2f(m4[j]));
      o[j] = pk2(lo2f(xr[kk][j]) + lo2f(a), hi2f(xr[kk][j]) + hi2f(a));
    }
    *(u32x4*)(orow + 16 * kk) = o;
  }
}

extern "C" void kernel_launch(void* const* d_in, const int* in_sizes, int n_in, void* d_out, int out_size,
                              void* d_ws, size_t ws_size, hipStream_t stream) {
  const u16* q = (const u16*)d_in[0];
  const u16* k = (const u16*)d_in[1];
  const u16* v = (const u16*)d_in[2];
  const u16* shk = (const u16*)d_in[3];
  const u16* kf = (const u16*)d_in[4];
  const u16* sD = (const u16*)d_in[5];
  const u16* wg = (const u16*)d_in[7];
  const u16* wu = (const u16*)d_in[8];
  const u16* wd = (const u16*)d_in[9];
  const u16* c = (const u16*)d_in[10];
  const u16* wm = (const u16*)d_in[11];
  char* ws = (char*)d_ws;
  (void)hipFuncSetAttribute((const void*)k1_conv, hipFuncAttributeMaxDynamicSharedMemorySize, K1_LDS);
  (void)hipFuncSetAttribute((const void*)k2_gemm1, hipFuncAttributeMaxDynamicSharedMemorySize, K2_LDS);
  (void)hipFuncSetAttribute((const void*)k3_gemm2, hipFuncAttributeMaxDynamicSharedMemorySize, K3_LDS);
  k0_prep<<<K0_NA + K0_NB + K0_NC, 256, 0, stream>>>(q, k, v, shk, c, wm, ws);
  k1_conv<<<256, 512, K1_LDS, stream>>>(kf, sD, wg, wu, wd, ws);
  k2_gemm1<<<256, 256, K2_LDS, stream>>>(ws);
  k3_gemm2<<<256, 256, K3_LDS, stream>>>(ws, (u16*)d_out);
}
```

```cpp
#include <hip/hip_runtime.h>
#include <stdint.h>

typedef unsigned short u16;
typedef unsigned int u32;
typedef __attribute__((ext_vector_type(8))) short bf16x8;
typedef __attribute__((ext_vector_type(4))) short s16x4;
typedef __attribute__((ext_vector_type(4))) float f32x4;
typedef __attribute__((ext_vector_type(2))) float f32x2;
typedef __attribute__((ext_vector_type(16))) float f32x16;
typedef __attribute__((ext_vector_type(4))) unsigned u32x4;
typedef __attribute__((ext_vector_type(2))) unsigned u32x2;
typedef __attribute__((ext_vector_type(2))) __bf16 bf2;
typedef __attribute__((address_space(3))) s16x4* lds_s4p;

#define DI __device__ __forceinline__

constexpr int S = 2048, D = 512, F = 1792;

DI float lo2f(u32 x) { return __uint_as_float(x << 16); }
DI float hi2f(u32 x) { return __uint_as_float(x & 0xffff0000u); }
DI u32 pk2(float a, float b) {
  f32x2 v = {a, b};
  return __builtin_bit_cast(u32, __builtin_convertvector(v, bf2));
}
DI float rb(float a) { return lo2f(pk2(a, a)); }
DI float dot2(u32 a, u32 b, float c) {
  return __builtin_amdgcn_fdot2_f32_bf16(__builtin_bit_cast(bf2, a), __builtin_bit_cast(bf2, b), c, false);
}

DI float red_8_16_32(float x) {
  x += __uint_as_float(__builtin_amdgcn_update_dpp(0u, __float_as_uint(x), 0x128, 0xf, 0xf, false));
  auto a = __builtin_amdgcn_permlane16_swap(__float_as_uint(x), __float_as_uint(x), false, false);
  x = __uint_as_float(a[0]) + __uint_as_float(a[1]);
  auto b = __builtin_amdgcn_permlane32_swap(__float_as_uint(x), __float_as_uint(x), false, false);
  return __uint_as_float(b[0]) + __uint_as_float(b[1]);
}
DI float relu(float x) {
  float r;
  asm("v_max_f32 %0, 0, %1" : "=v"(r) : "v"(x));
  return r;
}

#define STAMP_DECL
#define STAMP(kid, i)
#define STAMP_FLUSH(kid)
#define SCHED_FENCE() __builtin_amdgcn_sched_barrier(0)
#define RAWBAR()                                          \
  do {                                                    \
    __builtin_amdgcn_sched_barrier(0);                    \
    asm volatile("s_waitcnt lgkmcnt(0)" ::: "memory");    \
    __builtin_amdgcn_s_barrier();                         \
    asm volatile("" ::: "memory");                        \
    __builtin_amdgcn_sched_barrier(0);                    \
  } while (0)
#define SGB(mask, n) __builtin_amdgcn_sched_group_barrier(mask, n, 0)

constexpr size_t WS_MOD = 0;
constexpr size_t WS_OPC = WS_MOD + 3072 * 4;
constexpr size_t WS_SBB = WS_OPC + 1024 * 4;
constexpr size_t WS_STATS = 32768;
constexpr size_t WS_URT = 65536;
constexpr size_t WS_QT2 = WS_URT + 2u * S * D;
constexpr size_t WS_X1F = WS_QT2 + 2u * S * D;
constexpr size_t WS_W1F = WS_X1F + 2u * S * D;
constexpr size_t W1F_CB = 32 * 7 * 1024;
constexpr size_t WS_WDF = WS_W1F + 16 * W1F_CB;
constexpr size_t WS_HHF = WS_WDF + 2u * F * D;
constexpr size_t WS_FLG = WS_HHF + 2u * S * F;
constexpr size_t WS_CSP = WS_FLG + 1024 * 64;
constexpr size_t WS_ST2 = WS_CSP + 16 * 8 * 224 * 8;
constexpr size_t WS_END = WS_ST2 + 2048 * 8;

constexpr int K0_NA = 256, K0_NB = 128, K0_NC = 96;

__global__ __launch_bounds__(256) void k0_prep(const u16* __restrict__ q, const u16* __restrict__ k,
                                               const u16* __restrict__ v, const u16* __restrict__ shk,
                                               const u16* __restrict__ cc, const u16* __restrict__ wmod,
                                               char* __restrict__ ws) {
  __shared__ __attribute__((aligned(16))) u32 sm[4096];
  const int t = threadIdx.x;
  const int b0 = blockIdx.x;
  const int b = b0 < K0_NC ? b0 + K0_NA + K0_NB : b0 < K0_NC + K0_NB ? b0 - K0_NC + K0_NA : b0 - K0_NC - K0_NB;
  STAMP_DECL
  STAMP(0, 0);
  if (b < K0_NA) {
    const int sb = b >> 3, db = b & 7;
    const int rp = t >> 3, ci = t & 7;
    const int s0 = 64 * sb + 2 * rp, ch0 = 64 * db + 8 * ci;
    u32x4 kr[5], vr[2], qr[2], wr[4];
#pragma unroll
    for (int i = 0; i < 5; ++i) {
      int s = s0 - 3 + i;
      kr[i] = *(const u32x4*)(k + (size_t)(s < 0 ? 0 : s) * D + ch0);
    }
#pragma unroll
    for (int i = 0; i < 2; ++i) {
      vr[i] = *(const u32x4*)(v + (size_t)(s0 + i) * D + ch0);
      qr[i] = *(const u32x4*)(q + (size_t)(s0 + i) * D + ch0);
    }
#pragma unroll
    for (int i = 0; i < 4; ++i) wr[i] = *(const u32x4*)(shk + (size_t)ch0 * 4 + i * 8);
    SCHED_FENCE();
#pragma unroll
    for (int i = 0; i < 3; ++i) {
      const bool neg = (s0 - 3 + i) < 0;
#pragma unroll
      for (int j = 0; j < 4; ++j) kr[i][j] = neg ? 0u : kr[i][j];
    }
    u32* Tu = sm;
    u32* Tq = sm + 2048;
    const int x = 31 - rp;
#pragma unroll
    for (int j = 0; j < 4; ++j) {
      int pr = 4 * ci + j;
      int xs = 2 * rp;
      u32x2 val = {qr[0][j], qr[1][j]};
      *(u32x2*)&Tq[pr * 64 + ((((xs >> 2) ^ (2 * ci)) << 2) | (xs & 3))] = val;
    }
#pragma unroll
    for (int j = 0; j < 8; ++j) {
      u32 w01 = wr[j >> 1][(j & 1) * 2], w23 = wr[j >> 1][(j & 1) * 2 + 1];
      float w0 = lo2f(w01), w1 = hi2f(w01), w2 = lo2f(w23), w3 = hi2f(w23);
      float kk[5];
#pragma unroll
      for (int i = 0; i < 5; ++i) kk[i] = (j & 1) ? hi2f(kr[i][j >> 1]) : lo2f(kr[i][j >> 1]);
      u32 p = pk2(kk[0] * w3, kk[1] * w3);
      float a0 = lo2f(p), a1 = hi2f(p);
      p = pk2(kk[1] * w2, kk[2] * w2);
      p = pk2(a0 + lo2f(p), a1 + hi2f(p));
      a0 = lo2f(p); a1 = hi2f(p);
      p = pk2(kk[2] * w1, kk[3] * w1);
      p = pk2(a0 + lo2f(p), a1 + hi2f(p));
      a0 = lo2f(p); a1 = hi2f(p);
      p = pk2(kk[3] * w0, kk[4] * w0);
      p = pk2(a0 + lo2f(p), a1 + hi2f(p));
      a0 = lo2f(p); a1 = hi2f(p);
      float v0 = (j & 1) ? hi2f(vr[0][j >> 1]) : lo2f(vr[0][j >> 1]);
      float v1 = (j & 1) ? hi2f(vr[1][j >> 1]) : lo2f(vr[1][j >> 1]);
      u32 u = pk2(a1 * v1, a0 * v0);
      int ch = 8 * ci + j;
      Tu[ch * 32 + ((((x >> 2) ^ ci) << 2) | (x & 3))] = u;
    }
    STAMP(0, 1);
    __syncthreads();
    u16* urT = (u16*)(ws + WS_URT);
    u32* qT2 = (u32*)(ws + WS_QT2);
    const int tbase = S - 64 * (sb + 1);
#pragma unroll
    for (int i = 0; i < 2; ++i) {
      int idx = t + 256 * i;
      int ch = idx >> 3, c8 = idx & 7;
      u32x4 val = *(const u32x4*)&Tu[ch * 32 + ((c8 ^ (ch >> 3)) << 2)];
      *(u32x4*)(urT + (size_t)(64 * db + ch) * S + tbase + c8 * 8) = val;
      int pr = idx >> 4, c16 = idx & 15;
      u32x4 qv = *(const u32x4*)&Tq[pr * 64 + ((c16 ^ (2 * (pr >> 2))) << 2)];
      *(u32x4*)(qT2 + (size_t)(32 * db + pr) * S + 64 * sb + c16 * 4) = qv;
    }
  } else if (b < K0_NA + K0_NB) {
    const int w = t >> 6, lane = t & 63;
    const int r0 = (b - K0_NA) * 16 + w * 4;
    u32x4 xr[4];
#pragma unroll
    for (int i = 0; i < 4; ++i) xr[i] = *(const u32x4*)(q + (size_t)(r0 + i) * D + lane * 8);
    SCHED_FENCE();
    float sum[4];
#pragma unroll
    for (int i = 0; i < 4; ++i) {
      float a = 0.f;
#pragma unroll
      for (int j = 0; j < 4; ++j) a += lo2f(xr[i][j]) + hi2f(xr[i][j]);
      sum[i] = a;
    }
    STAMP(0, 1);
#pragma unroll
    for (int o = 32; o; o >>= 1)
#pragma unroll
      for (int i = 0; i < 4; ++i) sum[i] += __shfl_xor(sum[i], o);
    float sq[4];
#pragma unroll
    for (int i = 0; i < 4; ++i) {
      float m = sum[i] * (1.f / D), a = 0.f;
#pragma unroll
      for (int j = 0; j < 4; ++j) {
        float d0 = lo2f(xr[i][j]) - m, d1 = hi2f(xr[i][j]) - m;
        a += d0 * d0 + d1 * d1;
      }
      sq[i] = a;
    }
#pragma unroll
    for (int o = 32; o; o >>= 1)
#pragma unroll
      for (int i = 0; i < 4; ++i) sq[i] += __shfl_xor(sq[i], o);
    if (lane < 4) {
      float sm_ = lane == 0 ? sum[0] : lane == 1 ? sum[1] : lane == 2 ? sum[2] : sum[3];
      float sq_ = lane == 0 ? sq[0] : lane == 1 ? sq[1] : lane == 2 ? sq[2] : sq[3];
      float mb = rb(sm_ * (1.f / D));
      float sd = rb(sqrtf(rb(rb(sq_ * (1.f / D)) + 1e-5f)));
      f32x2 o = {mb, 1.0f / sd};
      ((f32x2*)(ws + WS_STATS))[r0 + lane] = o;
    }
  } else {
    const int cbi = b - K0_NA - K0_NB;
    const int cb = (cbi & 7) * 12 + (cbi >> 3);
    const int kq = t >> 2, cq = t & 3;
    u32x4 wv[8];
    u32x4 cv = *(const u32x4*)(cc + 8 * kq);
#pragma unroll
    for (int i = 0; i < 8; ++i) wv[i] = *(const u32x4*)(wmod + (size_t)(8 * kq + i) * (6 * D) + 32 * cb + 8 * cq);
    SCHED_FENCE();
    float acc[8];
#pragma unroll
    for (int j = 0; j < 8; ++j) acc[j] = 0.f;
#pragma unroll
    for (int i = 0; i < 8; ++i) {
      float x = (i & 1) ? hi2f(cv[i >> 1]) : lo2f(cv[i >> 1]);
      float sl = rb(x / (1.f + expf(-x)));
#pragma unroll
      for (int j = 0; j < 4; ++j) {
        acc[2 * j] += sl * lo2f(wv[i][j]);
        acc[2 * j + 1] += sl * hi2f(wv[i][j]);
      }
    }
    STAMP(0, 1);
#pragma unroll
    for (int o = 4; o < 64; o <<= 1)
#pragma unroll
      for (int j = 0; j < 8; ++j) acc[j] += __shfl_xor(acc[j], o);
    float* red = (float*)sm;
    const int w = t >> 6, lane = t & 63;
    if (lane < 4) {
#pragma unroll
      for (int j = 0; j < 8; ++j) red[w * 32 + lane * 8 + j] = acc[j];
    }
    __syncthreads();
    if (t < 32) {
      float r = rb(red[t] + red[32 + t] + red[64 + t] + red[96 + t]);
      int n = 32 * cb + t;
      ((float*)(ws + WS_MOD))[n] = r;
      int seg = n >> 9, d = n & 511;
      if (seg == 1) ((float*)(ws + WS_OPC))[d] = rb(1.f + r);
      if (seg == 4) ((float*)(ws + WS_OPC))[512 + d] = rb(1.f + r);
      if (seg == 3) ((u16*)(ws + WS_SBB))[d] = (u16)(__float_as_uint(r) >> 16);
    }
  }
  STAMP(0, 2);
  STAMP_FLUSH(b < K0_NA ? 0 : b < K0_NA + K0_NB ? 4 : 5);
}

constexpr int K1_CS = 4160;
constexpr int K1_FC = 0;
constexpr int K1_U = 66560;
constexpr int K1_PART = K1_U + 2 * 4608;
constexpr int K1_WSTW = 16 * 288 + 16 * 144;
constexpr int K1_WST = K1_PART + 36864;
constexpr int K1_RED = K1_WST + 4 * K1_WSTW;
constexpr int K1_SC = K1_RED + 8192;
constexpr int K1_LDS = K1_SC + 512;

DI constexpr int conv_n(int i) {
  return i < 8 ? 0 : i < 15 ? 1 : i < 21 ? 2 : i < 26 ? 3 : i < 30 ? 4 : i == 30 ? 5 : i == 31 ? 7 : i < 34 ? 5 : 6;
}
DI constexpr int conv_d(int i) {
  return i < 8 ? i : i < 15 ? i - 8 : i < 21 ? i - 15 : i < 26 ? i - 21 : i < 30 ? i - 26 : i < 32 ? 0 : i < 34 ? i - 31 : i - 34;
}

template <bool CONV>
DI void k1_body(const u16* __restrict__ kf, const u16* __restrict__ ssmD, const u16* __restrict__ wg,
                const u16* __restrict__ wu, const u16* __restrict__ wd, char* __restrict__ ws, char* sm, const int w) {
  const int b = blockIdx.x, t = threadIdx.x;
  STAMP_DECL
  STAMP(1, 0);
  const int xcd = b & 7, bi = b >> 3;
  const int p = xcd * 32 + bi;
  const int lane = t & 63;
  const int ci = t & 255;
  const bool has_gu = bi < 28;
  const int it = xcd * 28 + (has_gu ? bi : 0);
  const int slab = it >> 3, kc = it & 7;
  const int g = lane >> 4, h = lane >> 5, qq = (lane & 15) >> 2, pp = lane & 3;

  if (CONV) {
    u32x4 ka[2], kb[2], uu[2];
#pragma unroll
    for (int ch = 0; ch < 2; ++ch) {
      const u16* kfr = kf + (size_t)(2 * p + ch) * S;
      ka[ch] = *(const u32x4*)(kfr + (ci > 0 ? ci - 1 : 0) * 8);
      kb[ch] = *(const u32x4*)(kfr + ci * 8);
    }
#pragma unroll
    for (int ch = 0; ch < 2; ++ch)
      uu[ch] = *(const u32x4*)((const u16*)(ws + WS_URT) + (size_t)(2 * p + ch) * S + ci * 8);
    const u32 dpair = ((const u32*)ssmD)[p];
    SCHED_FENCE();
#pragma unroll
    for (int ch = 0; ch < 2; ++ch) {
      u32 wv[8] = {ka[ch][0], ka[ch][1], ka[ch][2], ka[ch][3], kb[ch][0], kb[ch][1], kb[ch][2], kb[ch][3]};
#pragma unroll
      for (int i = 0; i < 4; ++i) wv[i] = ci > 0 ? wv[i] : 0u;
      u32 od[7];
#pragma unroll
      for (int i = 0; i < 7; ++i) od[i] = __builtin_amdgcn_alignbit(wv[i + 1], wv[i], 16);
      char* base = sm + K1_FC + ch * (8 * K1_CS) + (ci + 1) * 16;
#pragma unroll
      for (int r = 0; r < 8; ++r) {
        const int sg = r + 1;
        u32x4 val;
        if (sg & 1) {
          const int k0 = (sg - 1) / 2;
          val = (u32x4){od[k0], od[k0 + 1], od[k0 + 2], od[k0 + 3]};
        } else {
          const int k0 = sg / 2;
          val = (u32x4){wv[k0], wv[k0 + 1], wv[k0 + 2], wv[k0 + 3]};
        }
        *(u32x4*)(base + r * K1_CS) = val;
      }
    }
#pragma unroll
    for (int ch = 0; ch < 2; ++ch) *(u32x4*)(sm + K1_U + ch * 4608 + ci * 16) = uu[ch];
    const int cch = w >> 1, c0 = 4 * (w & 1);
    const int l15 = lane & 15;
    const char* fb = sm + K1_FC + cch * (8 * K1_CS) + (l15 & 7) * K1_CS + 2 * ((l15 & 8) + 8 * g) + 64 * c0;
    const char* gb = sm + K1_U + cch * 4608 + 2 * (2032 - 256 * 7 - 16 * l15 + 8 * g) + 64 * c0;
    u32x4 df;
    {
      const u32 dv = c0 == 0 ? (cch ? (dpair >> 16) : (dpair & 0xffffu)) : 0u;
      const int kap = 15 - l15 - 8 * g;
#pragma unroll
      for (int j = 0; j < 4; ++j) df[j] = (kap == 2 * j) ? dv : (kap == 2 * j + 1) ? (dv << 16) : 0u;
    }
    const unsigned wo1 = ((unsigned)(16 * w + (lane >> 3)) * F + 8 * (lane & 7)) * 2;
    const unsigned wo2 = ((unsigned)(16 * w + (lane >> 3)) * D + 8 * (lane & 7)) * 2;
    const char* wgb = (const char*)(wg + (size_t)(64 * kc) * F + 64 * slab);
    const char* wub = (const char*)(wu + (size_t)(64 * kc) * F + 64 * slab);
    const char* wdb = (const char*)(wd + (size_t)(64 * slab) * D + 64 * kc);
    asm volatile("" : "+v"(df));
    STAMP(1, 1);
    RAWBAR();
    STAMP(1, 2);
    f32x4 acc[8];
    bf16x8 Fr[2][8], Gr[2][8];
#pragma unroll
    for (int n = 0; n < 8; ++n) Gr[0][n] = *(const bf16x8*)(gb + 512 * (7 - n));
#pragma unroll
    for (int n = 0; n < 8; ++n) Fr[0][n] = *(const bf16x8*)(fb + 512 * n);
    SCHED_FENCE();
    u32x4 wl[6];
    wl[0] = *(const u32x4*)(wgb + wo1);
    wl[1] = *(const u32x4*)(wgb + 8 * F * 2 + wo1);
    wl[2] = *(const u32x4*)(wub + wo1);
    wl[3] = *(const u32x4*)(wub + 8 * F * 2 + wo1);
    wl[4] = *(const u32x4*)(wdb + wo2);
    wl[5] = *(const u32x4*)(wdb + 8 * D * 2 + wo2);
    SCHED_FENCE();
#pragma unroll
    for (int m = 0; m < 8; ++m)
      acc[m] = __builtin_amdgcn_mfma_f32_16x16x32_bf16(__builtin_bit_cast(bf16x8, df), Gr[0][m],
                                                       (f32x4){0.f, 0.f, 0.f, 0.f}, 0, 0, 0);
    SCHED_FENCE();
#pragma unroll
    for (int c = 0; c < 4; ++c) {
      const int cur = c & 1, nxt = cur ^ 1;
#pragma unroll
      for (int i = 0; i < 36; ++i) {
        const int n = conv_n(i), dl = conv_d(i);
        acc[n + dl] = __builtin_amdgcn_mfma_f32_16x16x32_bf16(Fr[cur][n], Gr[cur][dl], acc[n + dl], 0, 0, 0);
        if (c < 3 && (i & 1) && i < 32) {
          const int j = i >> 1;
          if (j == 0) Fr[nxt][0] = *(const bf16x8*)(fb + 64 * (c + 1));
          else if (j <= 8) Gr[nxt][j - 1] = *(const bf16x8*)(gb + 512 * (8 - j) + 64 * (c + 1));
          else Fr[nxt][j - 8] = *(const bf16x8*)(fb + 512 * (j - 8) + 64 * (c + 1));
        }
        if (i & 1) SCHED_FENCE();
      }
    }
    float* part = (float*)(sm + K1_PART) + w * (8 * 288) + g * 72 + l15 * 4;
#pragma unroll
    for (int m = 0; m < 8; ++m) *(f32x4*)(part + m * 288) = acc[m];
    STAMP(1, 3);
    RAWBAR();
    STAMP(1, 4);
    SCHED_FENCE();
    char* wst = sm + K1_WST + w * K1_WSTW;
    {
      const int row = lane >> 3, c16 = lane & 7;
      char* d1 = wst + row * 288 + c16 * 16;
      *(u32x4*)(d1) = wl[0];
      *(u32x4*)(d1 + 8 * 288) = wl[1];
      *(u32x4*)(d1 + 128) = wl[2];
      *(u32x4*)(d1 + 8 * 288 + 128) = wl[3];
      char* d2 = wst + 16 * 288 + row * 144 + c16 * 16;
      *(u32x4*)(d2) = wl[4];
      *(u32x4*)(d2 + 8 * 144) = wl[5];
    }
    __builtin_amdgcn_fence(__ATOMIC_RELEASE, "wavefront");
    __builtin_amdgcn_wave_barrier();
    __builtin_amdgcn_fence(__ATOMIC_ACQUIRE, "wavefront");
    if (has_gu) {
      const f32x4 sc0 = *(const f32x4*)(sm + K1_SC + (16 * w + 8 * h) * 4);
      const f32x4 sc1 = *(const f32x4*)(sm + K1_SC + (16 * w + 8 * h + 4) * 4);
      const u32x4 sbv = *(const u32x4*)(sm + K1_SC + 256 + (16 * w + 8 * h) * 2);
      const int ks = 4 * kc + w;
#pragma unroll
      for (int j = 0; j < 4; ++j) {
        const int mtg = 4 * slab + j;
        const char* a0 = wst + (8 * h + qq) * 288 + (g & 1) * 128 + j * 32 + pp * 8;
        s16x4 lo = __builtin_amdgcn_ds_read_tr16_b64_v4i16((lds_s4p)(a0));
        s16x4 hi = __builtin_amdgcn_ds_read_tr16_b64_v4i16((lds_s4p)(a0 + 4 * 288));
        u32x2 l2 = __builtin_bit_cast(u32x2, lo), h2 = __builtin_bit_cast(u32x2, hi);
        u32 wv[4] = {l2[0], l2[1], h2[0], h2[1]};
        u32x4 o;
        o[0] = pk2(lo2f(wv[0]) * sc0[0], hi2f(wv[0]) * sc0[1]);
        o[1] = pk2(lo2f(wv[1]) * sc0[2], hi2f(wv[1]) * sc0[3]);
        o[2] = pk2(lo2f(wv[2]) * sc1[0], hi2f(wv[2]) * sc1[1]);
        o[3] = pk2(lo2f(wv[3]) * sc1[2], hi2f(wv[3]) * sc1[3]);
        float cs = 0.f, bi2 = 0.f;
#pragma unroll
        for (int e = 0; e < 4; ++e) {
          cs = dot2(o[e], 0x3f803f80u, cs);
          bi2 = dot2(wv[e], sbv[e], bi2);
        }
        *(u32x4*)(ws + WS_W1F + (size_t)(mtg / 7) * W1F_CB + (size_t)ks * 7168 + (mtg % 7) * 1024 + lane * 16) = o;
        f32x2 r = {cs, bi2};
        *(f32x2*)(sm + K1_RED + ((w * 4 + j) * 64 + lane) * 8) = r;
      }
#pragma unroll
      for (int j = 0; j < 2; ++j) {
        const char* a0 = wst + 16 * 288 + (4 * h + qq) * 144 + j * 64 + (g & 1) * 32 + pp * 8;
        s16x4 lo = __builtin_amdgcn_ds_read_tr16_b64_v4i16((lds_s4p)(a0));
        s16x4 hi = __builtin_amdgcn_ds_read_tr16_b64_v4i16((lds_s4p)(a0 + 8 * 144));
        u32x2 l2 = __builtin_bit_cast(u32x2, lo), h2 = __builtin_bit_cast(u32x2, hi);
        u32x4 o = {l2[0], l2[1], h2[0], h2[1]};
        *(u32x4*)(ws + WS_WDF + ((size_t)(2 * kc + j) * 112 + 4 * slab + w) * 1024 + lane * 16) = o;
      }
      __builtin_amdgcn_fence(__ATOMIC_RELEASE, "workgroup");
      u32 old = 0;
      if (lane == 0) old = __hip_atomic_fetch_add((u32*)(sm + K1_SC + 384), 1u, __ATOMIC_RELAXED, __HIP_MEMORY_SCOPE_WORKGROUP);
      old = __builtin_amdgcn_readfirstlane(old);
      if (old == 3) {
        __builtin_amdgcn_fence(__ATOMIC_ACQUIRE, "workgroup");
        const f32x2* red = (const f32x2*)(sm + K1_RED);
#pragma unroll
        for (int jj = 0; jj < 2; ++jj) {
          const int j = 2 * jj + h, r31 = lane & 31;
          f32x2 rr = {0.f, 0.f};
#pragma unroll
          for (int i = 0; i < 4; ++i) rr += red[(i * 4 + j) * 64 + r31] + red[(i * 4 + j) * 64 + 32 + r31];
          const int mtg = 4 * slab + j;
          *(f32x2*)(ws + WS_CSP + (((size_t)(mtg / 7) * 8 + kc) * 224 + (mtg % 7) * 32 + r31) * 8) = rr;
        }
      }
    }
  } else {
    const float* mod = (const float*)(ws + WS_MOD);
    const float* opc = (const float*)(ws + WS_OPC);
    u32 qp[8];
    f32x2 st[8];
#pragma unroll
    for (int i = 0; i < 8; ++i) {
      qp[i] = ((const u32*)(ws + WS_QT2))[(size_t)p * S + ci + 256 * i];
      st[i] = ((const f32x2*)(ws + WS_STATS))[ci + 256 * i];
    }
    const char* sp = ci < 16 ? ws + WS_OPC + 2048 + kc * 256 + ci * 16 : ws + WS_SBB + kc * 128 + (ci & 7) * 16;
    const u32x4 scv = *(const u32x4*)sp;
    const float sa0 = mod[2 * p], sa1 = mod[2 * p + 1];
    const float oc0 = opc[2 * p], oc1 = opc[2 * p + 1];
    const float ga0 = mod[1024 + 2 * p], ga1 = mod[1024 + 2 * p + 1];
    SCHED_FENCE();
    {
      const u32x4 z = {0, 0, 0, 0};
      if (ci < 16) *(u32x4*)(sm + K1_FC + ci * K1_CS) = z;
      if (ci >= 64 && ci < 128) *(u32x4*)(sm + K1_U + ((ci >> 5) & 1) * 4608 + 4096 + (ci & 31) * 16) = z;
      if (ci == 128) *(u32x4*)(sm + K1_SC + 384) = z;
      if (ci >= 192 && ci < 196) *(u32*)(ws + WS_FLG + (size_t)(b * 4 + ci - 192) * 64) = 0u;
      if (ci >= 200 && ci < 204) *(u32x4*)(ws + WS_ST2 + (size_t)(b * 4 + ci - 200) * 16) = z;
    }
    float h0[8], h1[8];
#pragma unroll
    for (int i = 0; i < 8; ++i) {
      u32 x = pk2(lo2f(qp[i]) - st[i][0], hi2f(qp[i]) - st[i][0]);
      x = pk2(lo2f(x) * st[i][1], hi2f(x) * st[i][1]);
      x = pk2(lo2f(x) * oc0, hi2f(x) * oc1);
      x = pk2(lo2f(x) + sa0, hi2f(x) + sa1);
      h0[i] = lo2f(x);
      h1[i] = hi2f(x);
      asm volatile("" : "+v"(h0[i]), "+v"(h1[i]));
    }
    if (ci < 24) *(u32x4*)(sm + K1_SC + ci * 16) = scv;
    STAMP(1, 1);
    RAWBAR();
    STAMP(1, 2);
    RAWBAR();
    STAMP(1, 4);
    const float* part = (const float*)(sm + K1_PART) + ((ci >> 2) & 3) * 72 + ((ci >> 4) & 15) * 4 + (ci & 3);
    const int d0 = 2 * p;
    char* dst = ws + WS_X1F + ((size_t)(d0 >> 4) * 64 + 32 * ((d0 >> 3) & 1)) * 16 + 2 * (d0 & 7) +
                (size_t)(ci >> 5) * 32768 + (ci & 31) * 16;
#pragma unroll
    for (int i = 0; i < 8; ++i) {
      float y0 = part[i * 288] + part[2304 + i * 288];
      float y1 = part[2 * 2304 + i * 288] + part[3 * 2304 + i * 288];
      u32 z = pk2(y0, y1);
      u32 a = pk2(h0[i] * lo2f(z), h1[i] * hi2f(z));
      a = pk2(ga0 * lo2f(a), ga1 * hi2f(a));
      u32 x1 = pk2(lo2f(qp[i]) + lo2f(a), hi2f(qp[i]) + hi2f(a));
      *(u32*)(dst + (size_t)i * (8 * 32768)) = x1;
    }
  }
  STAMP(1, 5);
  STAMP_FLUSH(1);
}

__global__ __launch_bounds__(512, 2) void k1_conv(const u16* __restrict__ kf, const u16* __restrict__ ssmD,
                                                  const u16* __restrict__ wg, const u16* __restrict__ wu,
                                                  const u16* __restrict__ wd, char* __restrict__ ws) {
  extern __shared__ __attribute__((aligned(16))) char sm[];
  const int w = __builtin_amdgcn_readfirstlane(threadIdx.x >> 6);
  if (w < 4) k1_body<true>(kf, ssmD, wg, wu, wd, ws, sm, w);
  else k1_body<false>(kf, ssmD, wg, wu, wd, ws, sm, w);
}

constexpr int K2_G = 28672;
constexpr int K2_NS = 4;
constexpr int K2_COR = K2_NS * K2_G;
constexpr int K2_FREE = K2_COR + 8192;
constexpr int K2_LDS = 163840;

#define GLOAD(dst, voff, sbase, imm) \
  asm volatile("global_load_dwordx4 %0, %1, %2 offset:" #imm : "=v"(dst) : "v"(voff), "s"(sbase) : "memory")
#define GDMA(voff, sbase, ldsa, imm)                                                               \
  asm volatile("s_mov_b32 m0, %2\n\ts_nop 0\n\tglobal_load_lds_dwordx4 %0, %1 offset:" #imm       \
               :: "v"(voff), "s"(sbase), "s"(ldsa) : "memory")

DI constexpr int wd_lds_off(int q, int w) {
  return q < 10 ? K2_FREE + w * 10240 + q * 1024
       : q < 17 ? 0 * K2_G + w * 7168 + (q - 10) * 1024
       : q < 24 ? 1 * K2_G + w * 7168 + (q - 17) * 1024
                : 2 * K2_G + w * 7168 + (q - 24) * 1024;
}

__global__ __launch_bounds__(256, 1) __attribute__((amdgpu_waves_per_eu(1, 1))) void k23_mlp(char* __restrict__ ws,
                                                                                             u16* __restrict__ out) {
  extern __shared__ __attribute__((aligned(16))) char sm[];
  const int b = blockIdx.x, t = threadIdx.x;
  STAMP_DECL
  STAMP(2, 0);
  const int w = __builtin_amdgcn_readfirstlane(t >> 6), lane = t & 63;
  const int xcd = b & 7, idx = b >> 3;
  const int rbk = (xcd & 1) * 8 + (idx & 7);
  const int cbk = (xcd >> 1) * 4 + (idx >> 3);
  const int st = rbk * 4 + w;
  const int sbk = xcd * 4 + (idx & 3);
  const int nbk = idx >> 2;
  const char* wsrc = ws + WS_W1F + (size_t)cbk * W1F_CB + w * 7168;
  const char* asrc = ws + WS_X1F + (size_t)st * 32768;
  const char* dsrc = ws + WS_WDF + ((size_t)(2 * nbk) * 112 + 28 * w) * 1024;
  const unsigned lbase = (unsigned)(uintptr_t)(__attribute__((address_space(3))) char*)sm;
  const unsigned ldst = lbase + w * 7168;
  const unsigned voff = lane * 16;

  u32x4 act[32];
#define VM(g, j)                                                                                   \
  {                                                                                                \
    if ((j) == 0) GLOAD(act[4 * (g) + 0], voff, asrc + (g) * 4096, 0);                             \
    if ((j) == 1) GLOAD(act[4 * (g) + 1], voff, asrc + (g) * 4096, 1024);                          \
    if ((j) == 2) GLOAD(act[4 * (g) + 2], voff, asrc + (g) * 4096, 2048);                          \
    if ((j) == 3) GLOAD(act[4 * (g) + 3], voff, asrc + (g) * 4096, 3072);                          \
    if ((j) == 4) GDMA(voff, wsrc + (g) * K2_G, ldst + ((g) % K2_NS) * K2_G, 0);                   \
    if ((j) == 5) GDMA(voff, wsrc + (g) * K2_G, ldst + ((g) % K2_NS) * K2_G, 1024);                \
    if ((j) == 6) GDMA(voff, wsrc + (g) * K2_G, ldst + ((g) % K2_NS) * K2_G, 2048);                \
    if ((j) == 7) GDMA(voff, wsrc + (g) * K2_G, ldst + ((g) % K2_NS) * K2_G, 3072);                \
    if ((j) == 8) GDMA(voff, wsrc + (g) * K2_G + 4096, ldst + ((g) % K2_NS) * K2_G + 4096, 0);     \
    if ((j) == 9) GDMA(voff, wsrc + (g) * K2_G + 4096, ldst + ((g) % K2_NS) * K2_G + 4096, 1024);  \
    if ((j) == 10) GDMA(voff, wsrc + (g) * K2_G + 4096, ldst + ((g) % K2_NS) * K2_G + 4096, 2048); \
  }
#define VMALL(g) { VM(g,4) VM(g,0) VM(g,5) VM(g,6) VM(g,7) VM(g,8) VM(g,9) VM(g,10) VM(g,1) VM(g,2) VM(g,3) }
#define WDM(q) { if ((q) >= 0 && (q) < 30) GDMA(voff, dsrc + ((q) & 1) * (112 * 1024) + ((q) >> 1) * 1024, lbase + wd_lds_off((q) < 0 ? 0 : (q), w), 0); }
  u32x4 sx[2];
  u32x2 cp[8];
  {
    const char* sbase = ws + WS_X1F + (size_t)(rbk * 4 + (cbk >> 2)) * 32768 + (size_t)w * 8192 + (cbk & 3) * 128;
    const unsigned so = (lane >> 3) * 512 + (lane & 7) * 16;
    GLOAD(sx[0], so, sbase, 0);
    GLOAD(sx[1], so, sbase + 4096, 0);
    const char* cbase = ws + WS_CSP + (size_t)cbk * (8 * 224 * 8);
    const unsigned co = (t < 224 ? t : 0) * 8;
#pragma unroll
    for (int i = 0; i < 8; ++i)
      asm volatile("global_load_dwordx2 %0, %1, %2" : "=v"(cp[i]) : "v"(co), "s"(cbase + i * 1792) : "memory");
  }
  VMALL(0) VMALL(1) VMALL(2)

  f32x16 acc[7];
#pragma unroll
  for (int m = 0; m < 7; ++m)
#pragma unroll
    for (int r = 0; r < 16; ++r) acc[m][r] = 0.f;
  const char* lsrc = sm + lane * 16;
  bf16x8 wf[2][7];

#define WAITG(g, n)                                                                                           \
  SCHED_FENCE();                                                                                              \
  asm volatile("s_waitcnt vmcnt(" #n ") lgkmcnt(0)"                                                           \
               : "+v"(act[4 * (g)]), "+v"(act[4 * (g) + 1]), "+v"(act[4 * (g) + 2]), "+v"(act[4 * (g) + 3]) \
               :                                                                                              \
               : "memory");                                                                                   \
  __builtin_amdgcn_s_barrier();                                                                               \
  asm volatile("" ::: "memory");                                                                              \
  SCHED_FENCE();
#define KOFF(i) ((i) == 32 ? K2_COR : (((i) >> 2) % K2_NS) * K2_G + ((i) & 3) * 7168)
#define RD(i, m) wf[(i) & 1][m] = *(const bf16x8*)(lsrc + KOFF(i) + (m) * 1024);
#define MM(i, m) acc[m] = __builtin_amdgcn_mfma_f32_32x32x16_bf16(wf[(i) & 1][m], __builtin_bit_cast(bf16x8, act[i]), acc[m], 0, 0, 0);
#define VX(vg, j0, q0, k) { if ((vg) >= 0) VM((vg) < 0 ? 0 : (vg), (j0) + (k)) else WDM((q0) < 0 ? -1 : (q0) + (k)) }
  u32x4 wr2[26];
#define WDR(r) { if ((r) >= 0 && (r) < 26) GLOAD(wr2[(r) < 0 ? 0 : (r) % 26], voff, dsrc + ((r) & 1) * (112 * 1024) + (15 + ((r) >> 1)) * 1024, 0); }
#define STEPR(i, vg, j0, q0, r0)                                                 \
  {                                                                              \
    MM(i, 0) RD((i) + 1, 0) RD((i) + 1, 1) SCHED_FENCE();                        \
    MM(i, 1) RD((i) + 1, 2) RD((i) + 1, 3) WDR(r0) SCHED_FENCE();                \
    MM(i, 2) RD((i) + 1, 4) RD((i) + 1, 5) WDR((r0) < 0 ? -1 : (r0) + 1) SCHED_FENCE(); \
    MM(i, 3) RD((i) + 1, 6) WDR((r0) < 0 ? -1 : (r0) + 2) SCHED_FENCE();         \
    MM(i, 4) VX(vg, j0, q0, 0) SCHED_FENCE();                                    \
    MM(i, 5) VX(vg, j0, q0, 1) SCHED_FENCE();                                    \
    MM(i, 6) VX(vg, j0, q0, 2) SCHED_FENCE();                                    \
  }
#define STEP(i, vg, j0, q0) STEPR(i, vg, j0, q0, -1)
  SCHED_FENCE();
  asm volatile("s_waitcnt vmcnt(33)"
               : "+v"(sx[0]), "+v"(sx[1]), "+v"(cp[0]), "+v"(cp[1]), "+v"(cp[2]), "+v"(cp[3]), "+v"(cp[4]), "+v"(cp[5]),
                 "+v"(cp[6]), "+v"(cp[7])
               :
               : "memory");
  {
    float sum = 0.f, sq = 0.f;
#pragma unroll
    for (int i = 0; i < 2; ++i)
#pragma unroll
      for (int j = 0; j < 4; ++j) {
        sum = dot2(sx[i][j], 0x3f803f80u, sum);
        sq = dot2(sx[i][j], sx[i][j], sq);
      }
    sum = red_8_16_32(sum);
    sq = red_8_16_32(sq);
    if (lane < 8) *(f32x2*)(sm + K2_COR + 7168 + (w * 8 + lane) * 8) = (f32x2){sum, sq};
    float cs = 0.f, bs = 0.f;
#pragma unroll
    for (int i = 0; i < 8; ++i) {
      cs += __uint_as_float(cp[i][0]);
      bs += __uint_as_float(cp[i][1]);
    }
    const float c0 = rb(cs), c1 = rb(cs - c0), c2 = rb(cs - c0 - c1);
    const float b0 = rb(bs), b1 = rb(bs - b0), b2 = rb(bs - b0 - b1);
    if (t < 224) {
      char* cd = sm + K2_COR + (t >> 5) * 1024 + (t & 31) * 16;
      *(u32x4*)cd = (u32x4){pk2(c0, c1), pk2(c2, b0), pk2(b1, b2), 0u};
      *(u32x4*)(cd + 512) = (u32x4){0u, 0u, 0u, 0u};
    }
  }
  WAITG(0, 22)
  STAMP(2, 1);
  RD(0, 0) RD(0, 1) RD(0, 2) RD(0, 3) RD(0, 4) RD(0, 5) RD(0, 6)
  if (w == 0) {
    if (lane < 8) {
      const f32x2* ps = (const f32x2*)(sm + K2_COR + 7168);
      const f32x2 r = ps[lane] + ps[8 + lane] + ps[16 + lane] + ps[24 + lane];
      const float mean = r[0] * (1.f / D);
      const float var = fmaxf(r[1] * (1.f / D) - mean * mean, 0.f);
      const float sdv = __builtin_amdgcn_sqrtf(rb(rb(var) + 1e-5f));
      const unsigned long long gr = (1ull << 32) | pk2(mean, sdv);
      __hip_atomic_store((unsigned long long*)(ws + WS_ST2) + rbk * 128 + cbk * 8 + lane, gr, __ATOMIC_RELAXED,
                         __HIP_MEMORY_SCOPE_AGENT);
    }
  }
  VM(3, 0) VM(3, 1)
  SCHED_FENCE();
  STEP(0, 3, 2, -1) STEP(1, 3, 5, -1) STEP(2, 3, 8, -1)
  WAITG(1, 22)
  STEP(3, 4, 0, -1) STEP(4, 4, 3, -1) STEP(5, 4, 6, -1) STEP(6, 4, 9, -1)
  WAITG(2, 22)
  STEP(7, 5, 0, -1) STEP(8, 5, 3, -1) STEP(9, 5, 6, -1) STEP(10, 5, 9, -1)
  WAITG(3, 22)
  STEP(11, 6, 0, -1) STEP(12, 6, 3, -1) STEP(13, 6, 6, -1) STEP(14, 6, 9, -1)
  WAITG(4, 22)
  STEP(15, 7, 0, -1) STEP(16, 7, 3, -1) STEP(17, 7, 6, -1) STEP(18, 7, 9, -1)
  u32x2 stg;
  const char* stp = ws + WS_ST2 + (size_t)st * 256;
  const unsigned sto = (lane & 31) * 8;
  asm volatile("global_load_dwordx2 %0, %1, %2 sc1" : "=v"(stg) : "v"(sto), "s"(stp) : "memory");
  WAITG(5, 23)
  STEP(19, -1, 0, 0) STEP(20, -1, 0, 3) STEP(21, -1, 0, 6) STEP(22, -1, 0, 9)
  WAITG(6, 24)
  STEP(23, -1, 0, 12) STEP(24, -1, 0, 15) STEP(25, -1, 0, 18) STEP(26, -1, 0, 21)
  WAITG(7, 24)
  asm volatile("" : "+v"(stg));
  for (int spins = 0; !__all(stg[1] == 1u) && spins < 20000; ++spins) {
    __builtin_amdgcn_s_sleep(1);
    asm volatile("global_load_dwordx2 %0, %1, %2 sc1\n\ts_waitcnt vmcnt(0)" : "=&v"(stg) : "v"(sto), "s"(stp) : "memory");
  }
  SCHED_FENCE();
  STEP(27, -1, 0, 24)
  bf16x8 tl[2][4];
#define RDT(m, k) tl[(m) & 1][k] = *(const bf16x8*)(lsrc + KOFF(29 + (k)) + (m) * 1024);
  const float mb = lo2f(stg[0]), sd = hi2f(stg[0]);
  float rs2;
  {
    MM(28, 0) RDT(0, 0) RDT(0, 1) SCHED_FENCE();
    MM(28, 1) RDT(0, 2) RDT(0, 3) SCHED_FENCE();
    MM(28, 2) WDM(27) SCHED_FENCE();
    MM(28, 3) WDM(28) SCHED_FENCE();
    MM(28, 4) WDM(29) SCHED_FENCE();
    MM(28, 5)
    const float rs = __builtin_amdgcn_rcpf(sd);
    rs2 = rs * rs;
    SCHED_FENCE();
    MM(28, 6) SCHED_FENCE();
  }
  STAMP(2, 2);
  const u32x4 cb4 = {pk2(-mb, -mb), pk2(-mb, sd), pk2(sd, sd), 0u};
  const auto wsr = __builtin_amdgcn_make_buffer_rsrc(ws, 0, 0x7fffffff, 0x00020000);
  const unsigned hoff = (unsigned)WS_HHF + ((unsigned)st * 112 + cbk * 7) * 1024;
  u32x4 eo;
#define EPJ(m, j)                                                                     \
  {                                                                                   \
    float u0 = acc[m][8 + 2 * (j)], u1 = acc[m][8 + 2 * (j) + 1];                     \
    asm volatile("" : "+v"(u0), "+v"(u1));                                            \
    float e0 = relu(acc[m][2 * (j)]) * u0 * rs2;                                      \
    float e1 = relu(acc[m][2 * (j) + 1]) * u1 * rs2;                                  \
    eo[j] = pk2(e0, e1);                                                              \
  }
#define EST(m) __builtin_amdgcn_raw_buffer_store_b128(eo, wsr, voff + (m) * 1024, hoff, 16);
#define MT(m, k, bop) acc[m] = __builtin_amdgcn_mfma_f32_32x32x16_bf16(tl[(m) & 1][k], __builtin_bit_cast(bf16x8, bop), acc[m], 0, 0, 0);
#define TAIL(m)                                                                                      \
  {                                                                                                  \
    MT(m, 0, act[29]) SCHED_FENCE();                                                                 \
    if ((m) < 6) { RDT((m) + 1, 0) RDT((m) + 1, 1) }                                                 \
    SCHED_FENCE();                                                                                   \
    MT(m, 1, act[30]) SCHED_FENCE();                                                                 \
    if ((m) < 6) { RDT((m) + 1, 2) RDT((m) + 1, 3) }                                                 \
    if ((m) > 0) EPJ((m) > 0 ? (m) - 1 : 0, 0)                                                       \
    SCHED_FENCE();                                                                                   \
    MT(m, 2, act[31]) SCHED_FENCE();                                                                 \
    if ((m) > 0) { EPJ((m) > 0 ? (m) - 1 : 0, 1) EPJ((m) > 0 ? (m) - 1 : 0, 2) }                     \
    SCHED_FENCE();                                                                                   \
    MT(m, 3, cb4) SCHED_FENCE();                                                                     \
    if ((m) > 0) { EPJ((m) > 0 ? (m) - 1 : 0, 3) EST((m) > 0 ? (m) - 1 : 0) }                        \
    SCHED_FENCE();                                                                                   \
  }
  TAIL(0) TAIL(1) TAIL(2) TAIL(3) TAIL(4) TAIL(5) TAIL(6)
  EPJ(6, 0) EPJ(6, 1) EPJ(6, 2) EPJ(6, 3) EST(6)
  asm volatile("s_waitcnt vmcnt(0)" ::: "memory");
  u32* flg = (u32*)(ws + WS_FLG);
  if (lane == 0) __hip_atomic_store(flg + (st * 16 + cbk) * 16, 1u, __ATOMIC_RELAXED, __HIP_MEMORY_SCOPE_AGENT);
  STAMP(2, 3);
  SCHED_FENCE();

#pragma unroll
  for (int r = 0; r < 26; ++r) wr2[r] = *(const u32x4*)(dsrc + (r & 1) * (112 * 1024) + (15 + (r >> 1)) * 1024 + voff);
  const int fn = w >> 1, fs = w & 1;
  const int h = lane >> 5;
  const int ncol = 64 * nbk + 32 * fn;
  u32x4 xr[2];
  f32x4 gv[4];
  {
    const char* xsrc = ws + WS_X1F + ((size_t)(2 * sbk + fs) * 32 + (ncol >> 4)) * 1024 + lane * 16;
    xr[0] = *(const u32x4*)(xsrc);
    xr[1] = *(const u32x4*)(xsrc + 1024);
    const float* gbp = (const float*)(ws + WS_MOD) + 5 * D + ncol + 8 * h;
    gv[0] = *(const f32x4*)(gbp);
    gv[1] = *(const f32x4*)(gbp + 4);
    gv[2] = *(const f32x4*)(gbp + 16);
    gv[3] = *(const f32x4*)(gbp + 20);
  }
  SCHED_FENCE();
  {
    const u32* fp = flg + ((2 * sbk + (lane & 1)) * 16 + 4 * w + ((lane >> 1) & 3)) * 16;
    for (int spins = 0; spins < 20000; ++spins) {
      u32 f = __hip_atomic_load(fp, __ATOMIC_RELAXED, __HIP_MEMORY_SCOPE_AGENT);
      if (__all(f == 1u)) break;
      __builtin_amdgcn_s_sleep(1);
    }
    __builtin_amdgcn_fence(__ATOMIC_ACQUIRE, "wavefront");
  }
  STAMP(2, 4);
  SCHED_FENCE();
  const unsigned hs = (unsigned)WS_HHF + ((unsigned)(2 * sbk) * 112 + 28 * w) * 1024;
  constexpr int WIN = 16;
  u32x4 hb[WIN][2];
#define HLD(i)                                                                                          \
  {                                                                                                     \
    hb[(i) % WIN][0] = __builtin_amdgcn_raw_buffer_load_b128(wsr, voff, hs + (i) * 1024, 16);           \
    hb[(i) % WIN][1] = __builtin_amdgcn_raw_buffer_load_b128(wsr, voff, hs + (112 + (i)) * 1024, 16);   \
  }
#pragma unroll
  for (int i = 0; i < WIN; ++i) HLD(i)
  SCHED_FENCE();
  f32x16 ac2[2][2];
#pragma unroll
  for (int a = 0; a < 2; ++a)
#pragma unroll
    for (int c = 0; c < 2; ++c)
#pragma unroll
      for (int r = 0; r < 16; ++r) ac2[a][c][r] = 0.f;
  bf16x8 wl2[2][2];
  wl2[0][0] = *(const bf16x8*)(lsrc + wd_lds_off(0, w));
  wl2[0][1] = *(const bf16x8*)(lsrc + wd_lds_off(1, w));
#pragma unroll
  for (int i = 0; i < 28; ++i) {
    if (i + 1 < 15) {
      wl2[(i + 1) & 1][0] = *(const bf16x8*)(lsrc + wd_lds_off(2 * (i + 1), w));
      wl2[(i + 1) & 1][1] = *(const bf16x8*)(lsrc + wd_lds_off(2 * (i + 1) + 1, w));
    }
#pragma unroll
    for (int a = 0; a < 2; ++a)
#pragma unroll
      for (int c = 0; c < 2; ++c)
        ac2[a][c] = __builtin_amdgcn_mfma_f32_32x32x16_bf16(i < 15 ? wl2[i & 1][a] : __builtin_bit_cast(bf16x8, wr2[i < 15 ? 0 : 2 * (i - 15) + a]),
                                                            __builtin_bit_cast(bf16x8, hb[i % WIN][c]), ac2[a][c], 0, 0, 0);
    if (i == 0) STAMP(2, 5);
    if (i + WIN < 28) HLD(i + WIN)
    SCHED_FENCE();
  }
  STAMP(2, 6);
#define PARK(w_, j) ((j) == 1 ? K2_FREE + (w_) * 10240 : (j) == 2 ? K2_FREE + (w_) * 10240 + 4096 : (w_) * 7168)
#pragma unroll
  for (int a = 0; a < 2; ++a)
#pragma unroll
    for (int c = 0; c < 2; ++c) {
      const int tile = a * 2 + c;
      if (tile != w) {
        const int j = (tile - w) & 3;
        char* dst = sm + PARK(w, j) + lane * 16;
#pragma unroll
        for (int r = 0; r < 4; ++r)
          *(f32x4*)(dst + r * 1024) =
              (f32x4){ac2[a][c][4 * r], ac2[a][c][4 * r + 1], ac2[a][c][4 * r + 2], ac2[a][c][4 * r + 3]};
      }
    }
  __syncthreads();
  f32x16 fin;
  if (w == 0) fin = ac2[0][0];
  else if (w == 1) fin = ac2[0][1];
  else if (w == 2) fin = ac2[1][0];
  else fin = ac2[1][1];
#pragma unroll
  for (int o = 1; o < 4; ++o) {
    const int ow = (w + o) & 3;
    const char* src = sm + PARK(ow, 4 - o) + lane * 16;
#pragma unroll
    for (int r = 0; r < 4; ++r) {
      f32x4 v = *(const f32x4*)(src + r * 1024);
#pragma unroll
      for (int e = 0; e < 4; ++e) fin[4 * r + e] += v[e];
    }
  }
  u32 pkd[8];
#pragma unroll
  for (int j = 0; j < 8; ++j) pkd[j] = pk2(fin[2 * j], fin[2 * j + 1]);
  const int srow = 64 * sbk + 32 * fs + (lane & 31);
  u16* orow = out + (size_t)srow * D + ncol + 8 * h;
#pragma unroll
  for (int kk = 0; kk < 2; ++kk) {
    auto r0 = __builtin_amdgcn_permlane32_swap(pkd[4 * kk], pkd[4 * kk + 2], false, false);
    auto r1 = __builtin_amdgcn_permlane32_swap(pkd[4 * kk + 1], pkd[4 * kk + 3], false, false);
    u32 m4[4] = {r0[0], r1[0], r0[1], r1[1]};
    u32x4 o;
#pragma unroll
    for (int j = 0; j < 4; ++j) {
      float g0 = gv[2 * kk + (j >> 1)][(j & 1) * 2], g1 = gv[2 * kk + (j >> 1)][(j & 1) * 2 + 1];
      u32 a = pk2(g0 * lo2f(m4[j]), g1 * hi2f(m4[j]));
      o[j] = pk2(lo2f(xr[kk][j]) + lo2f(a), hi2f(xr[kk][j]) + hi2f(a));
    }
    *(u32x4*)(orow + 16 * kk) = o;
  }
  STAMP(2, 7);
  STAMP_FLUSH(2);
}

extern "C" void kernel_launch(void* const* d_in, const int* in_sizes, int n_in, void* d_out, int out_size,
                              void* d_ws, size_t ws_size, hipStream_t stream) {
  const u16* q = (const u16*)d_in[0];
  const u16* k = (const u16*)d_in[1];
  const u16* v = (const u16*)d_in[2];
  const u16* shk = (const u16*)d_in[3];
  const u16* kf = (const u16*)d_in[4];
  const u16* sD = (const u16*)d_in[5];
  const u16* wg = (const u16*)d_in[7];
  const u16* wu = (const u16*)d_in[8];
  const u16* wd = (const u16*)d_in[9];
  const u16* c = (const u16*)d_in[10];
  const u16* wm = (const u16*)d_in[11];
  char* ws = (char*)d_ws;
  (void)hipFuncSetAttribute((const void*)k1_conv, hipFuncAttributeMaxDynamicSharedMemorySize, K1_LDS);
  (void)hipFuncSetAttribute((const void*)k23_mlp, hipFuncAttributeMaxDynamicSharedMemorySize, K2_LDS);
  k0_prep<<<K0_NA + K0_NB + K0_NC, 256, 0, stream>>>(q, k, v, shk, c, wm, ws);
  k1_conv<<<256, 512, K1_LDS, stream>>>(kf, sD, wg, wu, wd, ws);
  k23_mlp<<<256, 256, K2_LDS, stream>>>(ws, (u16*)d_out);
}
```

```cpp
#define STMODE 0
#include <hip/hip_runtime.h>
#include <stdint.h>

typedef unsigned short u16;
typedef unsigned int u32;
typedef __attribute__((ext_vector_type(8))) short bf16x8;
typedef __attribute__((ext_vector_type(4))) short s16x4;
typedef __attribute__((ext_vector_type(4))) float f32x4;
typedef __attribute__((ext_vector_type(2))) float f32x2;
typedef __attribute__((ext_vector_type(16))) float f32x16;
typedef __attribute__((ext_vector_type(4))) unsigned u32x4;
typedef __attribute__((ext_vector_type(2))) unsigned u32x2;
typedef __attribute__((ext_vector_type(2))) __bf16 bf2;
typedef __attribute__((address_space(3))) s16x4* lds_s4p;

#define DI __device__ __forceinline__

constexpr int S = 2048, D = 512, F = 1792;

DI float lo2f(u32 x) { return __uint_as_float(x << 16); }
DI float hi2f(u32 x) { return __uint_as_float(x & 0xffff0000u); }
DI u32 pk2(float a, float b) {
  f32x2 v = {a, b};
  return __builtin_bit_cast(u32, __builtin_convertvector(v, bf2));
}
DI float rb(float a) { return lo2f(pk2(a, a)); }
DI float dot2(u32 a, u32 b, float c) {
  return __builtin_amdgcn_fdot2_f32_bf16(__builtin_bit_cast(bf2, a), __builtin_bit_cast(bf2, b), c, false);
}

DI float red_8_16_32(float x) {
  x += __uint_as_float(__builtin_amdgcn_update_dpp(0u, __float_as_uint(x), 0x128, 0xf, 0xf, false));
  auto a = __builtin_amdgcn_permlane16_swap(__float_as_uint(x), __float_as_uint(x), false, false);
  x = __uint_as_float(a[0]) + __uint_as_float(a[1]);
  auto b = __builtin_amdgcn_permlane32_swap(__float_as_uint(x), __float_as_uint(x), false, false);
  return __uint_as_float(b[0]) + __uint_as_float(b[1]);
}
DI float relu(float x) {
  float r;
  asm("v_max_f32 %0, 0, %1" : "=v"(r) : "v"(x));
  return r;
}

DI void st16(void* p, u32x4 v) {
  *(u32x4*)p = v;
}

#define STAMP_DECL
#define STAMP(kid, i)
#define STAMP_FLUSH(kid)
#define SCHED_FENCE() __builtin_amdgcn_sched_barrier(0)
#define RAWBAR()                                          \
  do {                                                    \
    __builtin_amdgcn_sched_barrier(0);                    \
    asm volatile("s_waitcnt lgkmcnt(0)" ::: "memory");    \
    __builtin_amdgcn_s_barrier();                         \
    asm volatile("" ::: "memory");                        \
    __builtin_amdgcn_sched_barrier(0);                    \
  } while (0)
#define SGB(mask, n) __builtin_amdgcn_sched_group_barrier(mask, n, 0)

constexpr size_t WS_MOD = 0;
constexpr size_t WS_OPC = WS_MOD + 3072 * 4;
constexpr size_t WS_SBB = WS_OPC + 1024 * 4;
constexpr size_t WS_STATS = 32768;
constexpr size_t WS_URT = 65536;
constexpr size_t WS_QT2 = WS_URT + 2u * S * D;
constexpr size_t WS_X1F = WS_QT2 + 2u * S * D;
constexpr size_t WS_W1F = WS_X1F + 2u * S * D;
constexpr size_t W1F_CB = 32 * 7 * 1024;
constexpr size_t WS_WDF = WS_W1F + 16 * W1F_CB;
constexpr size_t WS_HHF = WS_WDF + 2u * F * D;
constexpr size_t WS_FLG = WS_HHF + 2u * S * F;
constexpr size_t WS_CSP = WS_FLG + 1024 * 64;
constexpr size_t WS_ST2 = WS_CSP + 16 * 8 * 224 * 8;
constexpr size_t WS_END = WS_ST2 + 2048 * 8;

constexpr int K0_NA = 256, K0_NB = 128, K0_NC = 96;

__global__ __launch_bounds__(256) void k0_prep(const u16* __restrict__ q, const u16* __restrict__ k,
                                               const u16* __restrict__ v, const u16* __restrict__ shk,
                                               const u16* __restrict__ cc, const u16* __restrict__ wmod,
                                               char* __restrict__ ws) {
  __shared__ __attribute__((aligned(16))) u32 sm[4096];
  const int t = threadIdx.x;
  const int b0 = blockIdx.x;
  const int b = b0 < K0_NC ? b0 + K0_NA + K0_NB : b0 < K0_NC + K0_NB ? b0 - K0_NC + K0_NA : b0 - K0_NC - K0_NB;
  STAMP_DECL
  STAMP(0, 0);
  if (b < K0_NA) {
    const int sb = b >> 3, db = b & 7;
    const int rp = t >> 3, ci = t & 7;
    const int s0 = 64 * sb + 2 * rp, ch0 = 64 * db + 8 * ci;
    u32x4 kr[5], vr[2], qr[2], wr[4];
#pragma unroll
    for (int i = 0; i < 5; ++i) {
      int s = s0 - 3 + i;
      kr[i] = *(const u32x4*)(k + (size_t)(s < 0 ? 0 : s) * D + ch0);
    }
#pragma unroll
    for (int i = 0; i < 2; ++i) {
      vr[i] = *(const u32x4*)(v + (size_t)(s0 + i) * D + ch0);
      qr[i] = *(const u32x4*)(q + (size_t)(s0 + i) * D + ch0);
    }
#pragma unroll
    for (int i = 0; i < 4; ++i) wr[i] = *(const u32x4*)(shk + (size_t)ch0 * 4 + i * 8);
    SCHED_FENCE();
#pragma unroll
    for (int i = 0; i < 3; ++i) {
      const bool neg = (s0 - 3 + i) < 0;
#pragma unroll
      for (int j = 0; j < 4; ++j) kr[i][j] = neg ? 0u : kr[i][j];
    }
    u32* Tu = sm;
    u32* Tq = sm + 2048;
    const int x = 31 - rp;
#pragma unroll
    for (int j = 0; j < 4; ++j) {
      int pr = 4 * ci + j;
      int xs = 2 * rp;
      u32x2 val = {qr[0][j], qr[1][j]};
      *(u32x2*)&Tq[pr * 64 + ((((xs >> 2) ^ (2 * ci)) << 2) | (xs & 3))] = val;
    }
#pragma unroll
    for (int j = 0; j < 8; ++j) {
      u32 w01 = wr[j >> 1][(j & 1) * 2], w23 = wr[j >> 1][(j & 1) * 2 + 1];
      float w0 = lo2f(w01), w1 = hi2f(w01), w2 = lo2f(w23), w3 = hi2f(w23);
      float kk[5];
#pragma unroll
      for (int i = 0; i < 5; ++i) kk[i] = (j & 1) ? hi2f(kr[i][j >> 1]) : lo2f(kr[i][j >> 1]);
      u32 p = pk2(kk[0] * w3, kk[1] * w3);
      float a0 = lo2f(p), a1 = hi2f(p);
      p = pk2(kk[1] * w2, kk[2] * w2);
      p = pk2(a0 + lo2f(p), a1 + hi2f(p));
      a0 = lo2f(p); a1 = hi2f(p);
      p = pk2(kk[2] * w1, kk[3] * w1);
      p = pk2(a0 + lo2f(p), a1 + hi2f(p));
      a0 = lo2f(p); a1 = hi2f(p);
      p = pk2(kk[3] * w0, kk[4] * w0);
      p = pk2(a0 + lo2f(p), a1 + hi2f(p));
      a0 = lo2f(p); a1 = hi2f(p);
      float v0 = (j & 1) ? hi2f(vr[0][j >> 1]) : lo2f(vr[0][j >> 1]);
      float v1 = (j & 1) ? hi2f(vr[1][j >> 1]) : lo2f(vr[1][j >> 1]);
      u32 u = pk2(a1 * v1, a0 * v0);
      int ch = 8 * ci + j;
      Tu[ch * 32 + ((((x >> 2) ^ ci) << 2) | (x & 3))] = u;
    }
    STAMP(0, 1);
    __syncthreads();
    u16* urT = (u16*)(ws + WS_URT);
    u32* qT2 = (u32*)(ws + WS_QT2);
    const int tbase = S - 64 * (sb + 1);
#pragma unroll
    for (int i = 0; i < 2; ++i) {
      int idx = t + 256 * i;
      int ch = idx >> 3, c8 = idx & 7;
      u32x4 val = *(const u32x4*)&Tu[ch * 32 + ((c8 ^ (ch >> 3)) << 2)];
      st16(urT + (size_t)(64 * db + ch) * S + tbase + c8 * 8, val);
      int pr = idx >> 4, c16 = idx & 15;
      u32x4 qv = *(const u32x4*)&Tq[pr * 64 + ((c16 ^ (2 * (pr >> 2))) << 2)];
      st16(qT2 + (size_t)(32 * db + pr) * S + 64 * sb + c16 * 4, qv);
    }
  } else if (b < K0_NA + K0_NB) {
    const int w = t >> 6, lane = t & 63;
    const int r0 = (b - K0_NA) * 16 + w * 4;
    u32x4 xr[4];
#pragma unroll
    for (int i = 0; i < 4; ++i) xr[i] = *(const u32x4*)(q + (size_t)(r0 + i) * D + lane * 8);
    SCHED_FENCE();
    float sum[4];
#pragma unroll
    for (int i = 0; i < 4; ++i) {
      float a = 0.f;
#pragma unroll
      for (int j = 0; j < 4; ++j) a += lo2f(xr[i][j]) + hi2f(xr[i][j]);
      sum[i] = a;
    }
    STAMP(0, 1);
#pragma unroll
    for (int o = 32; o; o >>= 1)
#pragma unroll
      for (int i = 0; i < 4; ++i) sum[i] += __shfl_xor(sum[i], o);
    float sq[4];
#pragma unroll
    for (int i = 0; i < 4; ++i) {
      float m = sum[i] * (1.f / D), a = 0.f;
#pragma unroll
      for (int j = 0; j < 4; ++j) {
        float d0 = lo2f(xr[i][j]) - m, d1 = hi2f(xr[i][j]) - m;
        a += d0 * d0 + d1 * d1;
      }
      sq[i] = a;
    }
#pragma unroll
    for (int o = 32; o; o >>= 1)
#pragma unroll
      for (int i = 0; i < 4; ++i) sq[i] += __shfl_xor(sq[i], o);
    if (lane < 4) {
      float sm_ = lane == 0 ? sum[0] : lane == 1 ? sum[1] : lane == 2 ? sum[2] : sum[3];
      float sq_ = lane == 0 ? sq[0] : lane == 1 ? sq[1] : lane == 2 ? sq[2] : sq[3];
      float mb = rb(sm_ * (1.f / D));
      float sd = rb(sqrtf(rb(rb(sq_ * (1.f / D)) + 1e-5f)));
      f32x2 o = {mb, 1.0f / sd};
      ((f32x2*)(ws + WS_STATS))[r0 + lane] = o;
    }
  } else {
    const int cbi = b - K0_NA - K0_NB;
    const int cb = (cbi & 7) * 12 + (cbi >> 3);
    const int kq = t >> 2, cq = t & 3;
    u32x4 wv[8];
    u32x4 cv = *(const u32x4*)(cc + 8 * kq);
#pragma unroll
    for (int i = 0; i < 8; ++i) wv[i] = *(const u32x4*)(wmod + (size_t)(8 * kq + i) * (6 * D) + 32 * cb + 8 * cq);
    SCHED_FENCE();
    float acc[8];
#pragma unroll
    for (int j = 0; j < 8; ++j) acc[j] = 0.f;
#pragma unroll
    for (int i = 0; i < 8; ++i) {
      float x = (i & 1) ? hi2f(cv[i >> 1]) : lo2f(cv[i >> 1]);
      float sl = rb(x / (1.f + expf(-x)));
#pragma unroll
      for (int j = 0; j < 4; ++j) {
        acc[2 * j] += sl * lo2f(wv[i][j]);
        acc[2 * j + 1] += sl * hi2f(wv[i][j]);
      }
    }
    STAMP(0, 1);
#pragma unroll
    for (int o = 4; o < 64; o <<= 1)
#pragma unroll
      for (int j = 0; j < 8; ++j) acc[j] += __shfl_xor(acc[j], o);
    float* red = (float*)sm;
    const int w = t >> 6, lane = t & 63;
    if (lane < 4) {
#pragma unroll
      for (int j = 0; j < 8; ++j) red[w * 32 + lane * 8 + j] = acc[j];
    }
    __syncthreads();
    if (t < 32) {
      float r = rb(red[t] + red[32 + t] + red[64 + t] + red[96 + t]);
      int n = 32 * cb + t;
      ((float*)(ws + WS_MOD))[n] = r;
      int seg = n >> 9, d = n & 511;
      if (seg == 1) ((float*)(ws + WS_OPC))[d] = rb(1.f + r);
      if (seg == 4) ((float*)(ws + WS_OPC))[512 + d] = rb(1.f + r);
      if (seg == 3) ((u16*)(ws + WS_SBB))[d] = (u16)(__float_as_uint(r) >> 16);
    }
  }
  STAMP(0, 2);
  STAMP_FLUSH(b < K0_NA ? 0 : b < K0_NA + K0_NB ? 4 : 5);
}

constexpr int K1_CS = 4160;
constexpr int K1_FC = 0;
constexpr int K1_U = 66560;
constexpr int K1_PART = K1_U + 2 * 4608;
constexpr int K1_WSTW = 16 * 288 + 16 * 144;
constexpr int K1_WST = K1_PART + 36864;
constexpr int K1_RED = K1_WST + 4 * K1_WSTW;
constexpr int K1_SC = K1_RED + 8192;
constexpr int K1_LDS = K1_SC + 512;

DI constexpr int conv_n(int i) {
  return i < 8 ? 0 : i < 15 ? 1 : i < 21 ? 2 : i < 26 ? 3 : i < 30 ? 4 : i == 30 ? 5 : i == 31 ? 7 : i < 34 ? 5 : 6;
}
DI constexpr int conv_d(int i) {
  return i < 8 ? i : i < 15 ? i - 8 : i < 21 ? i - 15 : i < 26 ? i - 21 : i < 30 ? i - 26 : i < 32 ? 0 : i < 34 ? i - 31 : i - 34;
}

template <bool CONV>
DI void k1_body(const u16* __restrict__ kf, const u16* __restrict__ ssmD, const u16* __restrict__ wg,
                const u16* __restrict__ wu, const u16* __restrict__ wd, char* __restrict__ ws, char* sm, const int w) {
  const int b = blockIdx.x, t = threadIdx.x;
  STAMP_DECL
  STAMP(1, 0);
  const int xcd = b & 7, bi = b >> 3;
  const int p = xcd * 32 + bi;
  const int lane = t & 63;
  const int ci = t & 255;
  const bool has_gu = bi < 28;
  const int it = xcd * 28 + (has_gu ? bi : 0);
  const int slab = it >> 3, kc = it & 7;
  const int g = lane >> 4, h = lane >> 5, qq = (lane & 15) >> 2, pp = lane & 3;

  if (CONV) {
    u32x4 ka[2], kb[2], uu[2];
#pragma unroll
    for (int ch = 0; ch < 2; ++ch) {
      const u16* kfr = kf + (size_t)(2 * p + ch) * S;
      ka[ch] = *(const u32x4*)(kfr + (ci > 0 ? ci - 1 : 0) * 8);
      kb[ch] = *(const u32x4*)(kfr + ci * 8);
    }
#pragma unroll
    for (int ch = 0; ch < 2; ++ch)
      uu[ch] = *(const u32x4*)((const u16*)(ws + WS_URT) + (size_t)(2 * p + ch) * S + ci * 8);
    const u32 dpair = ((const u32*)ssmD)[p];
    SCHED_FENCE();
#pragma unroll
    for (int ch = 0; ch < 2; ++ch) {
      u32 wv[8] = {ka[ch][0], ka[ch][1], ka[ch][2], ka[ch][3], kb[ch][0], kb[ch][1], kb[ch][2], kb[ch][3]};
#pragma unroll
      for (int i = 0; i < 4; ++i) wv[i] = ci > 0 ? wv[i] : 0u;
      u32 od[7];
#pragma unroll
      for (int i = 0; i < 7; ++i) od[i] = __builtin_amdgcn_alignbit(wv[i + 1], wv[i], 16);
      char* base = sm + K1_FC + ch * (8 * K1_CS) + (ci + 1) * 16;
#pragma unroll
      for (int r = 0; r < 8; ++r) {
        const int sg = r + 1;
        u32x4 val;
        if (sg & 1) {
          const int k0 = (sg - 1) / 2;
          val = (u32x4){od[k0], od[k0 + 1], od[k0 + 2], od[k0 + 3]};
        } else {
          const int k0 = sg / 2;
          val = (u32x4){wv[k0], wv[k0 + 1], wv[k0 + 2], wv[k0 + 3]};
        }
        *(u32x4*)(base + r * K1_CS) = val;
      }
    }
#pragma unroll
    for (int ch = 0; ch < 2; ++ch) *(u32x4*)(sm + K1_U + ch * 4608 + ci * 16) = uu[ch];
    const int cch = w >> 1, c0 = 4 * (w & 1);
    const int l15 = lane & 15;
    const char* fb = sm + K1_FC + cch * (8 * K1_CS) + (l15 & 7) * K1_CS + 2 * ((l15 & 8) + 8 * g) + 64 * c0;
    const char* gb = sm + K1_U + cch * 4608 + 2 * (2032 - 256 * 7 - 16 * l15 + 8 * g) + 64 * c0;
    u32x4 df;
    {
      const u32 dv = c0 == 0 ? (cch ? (dpair >> 16) : (dpair & 0xffffu)) : 0u;
      const int kap = 15 - l15 - 8 * g;
#pragma unroll
      for (int j = 0; j < 4; ++j) df[j] = (kap == 2 * j) ? dv : (kap == 2 * j + 1) ? (dv << 16) : 0u;
    }
    const unsigned wo1 = ((unsigned)(16 * w + (lane >> 3)) * F + 8 * (lane & 7)) * 2;
    const unsigned wo2 = ((unsigned)(16 * w + (lane >> 3)) * D + 8 * (lane & 7)) * 2;
    const char* wgb = (const char*)(wg + (size_t)(64 * kc) * F + 64 * slab);
    const char* wub = (const char*)(wu + (size_t)(64 * kc) * F + 64 * slab);
    const char* wdb = (const char*)(wd + (size_t)(64 * slab) * D + 64 * kc);
    asm volatile("" : "+v"(df));
    STAMP(1, 1);
    RAWBAR();
    STAMP(1, 2);
    f32x4 acc[8];
    bf16x8 Fr[2][8], Gr[2][8];
#pragma unroll
    for (int n = 0; n < 8; ++n) Gr[0][n] = *(const bf16x8*)(gb + 512 * (7 - n));
#pragma unroll
    for (int n = 0; n < 8; ++n) Fr[0][n] = *(const bf16x8*)(fb + 512 * n);
    SCHED_FENCE();
    u32x4 wl[6];
    wl[0] = *(const u32x4*)(wgb + wo1);
    wl[1] = *(const u32x4*)(wgb + 8 * F * 2 + wo1);
    wl[2] = *(const u32x4*)(wub + wo1);
    wl[3] = *(const u32x4*)(wub + 8 * F * 2 + wo1);
    wl[4] = *(const u32x4*)(wdb + wo2);
    wl[5] = *(const u32x4*)(wdb + 8 * D * 2 + wo2);
    SCHED_FENCE();
#pragma unroll
    for (int m = 0; m < 8; ++m)
      acc[m] = __builtin_amdgcn_mfma_f32_16x16x32_bf16(__builtin_bit_cast(bf16x8, df), Gr[0][m],
                                                       (f32x4){0.f, 0.f, 0.f, 0.f}, 0, 0, 0);
    SCHED_FENCE();
#pragma unroll
    for (int c = 0; c < 4; ++c) {
      const int cur = c & 1, nxt = cur ^ 1;
#pragma unroll
      for (int i = 0; i < 36; ++i) {
        const int n = conv_n(i), dl = conv_d(i);
        acc[n + dl] = __builtin_amdgcn_mfma_f32_16x16x32_bf16(Fr[cur][n], Gr[cur][dl], acc[n + dl], 0, 0, 0);
        if (c < 3 && (i & 1) && i < 32) {
          const int j = i >> 1;
          if (j == 0) Fr[nxt][0] = *(const bf16x8*)(fb + 64 * (c + 1));
          else if (j <= 8) Gr[nxt][j - 1] = *(const bf16x8*)(gb + 512 * (8 - j) + 64 * (c + 1));
          else Fr[nxt][j - 8] = *(const bf16x8*)(fb + 512 * (j - 8) + 64 * (c + 1));
        }
        if (i & 1) SCHED_FENCE();
      }
    }
    float* part = (float*)(sm + K1_PART) + w * (8 * 288) + g * 72 + l15 * 4;
#pragma unroll
    for (int m = 0; m < 8; ++m) *(f32x4*)(part + m * 288) = acc[m];
    STAMP(1, 3);
    RAWBAR();
    STAMP(1, 4);
    SCHED_FENCE();
    char* wst = sm + K1_WST + w * K1_WSTW;
    {
      const int row = lane >> 3, c16 = lane & 7;
      char* d1 = wst + row * 288 + c16 * 16;
      *(u32x4*)(d1) = wl[0];
      *(u32x4*)(d1 + 8 * 288) = wl[1];
      *(u32x4*)(d1 + 128) = wl[2];
      *(u32x4*)(d1 + 8 * 288 + 128) = wl[3];
      char* d2 = wst + 16 * 288 + row * 144 + c16 * 16;
      *(u32x4*)(d2) = wl[4];
      *(u32x4*)(d2 + 8 * 144) = wl[5];
    }
    __builtin_amdgcn_fence(__ATOMIC_RELEASE, "wavefront");
    __builtin_amdgcn_wave_barrier();
    __builtin_amdgcn_fence(__ATOMIC_ACQUIRE, "wavefront");
    if (has_gu) {
      const f32x4 sc0 = *(const f32x4*)(sm + K1_SC + (16 * w + 8 * h) * 4);
      const f32x4 sc1 = *(const f32x4*)(sm + K1_SC + (16 * w + 8 * h + 4) * 4);
      const u32x4 sbv = *(const u32x4*)(sm + K1_SC + 256 + (16 * w + 8 * h) * 2);
      const int ks = 4 * kc + w;
#pragma unroll
      for (int j = 0; j < 4; ++j) {
        const int mtg = 4 * slab + j;
        const char* a0 = wst + (8 * h + qq) * 288 + (g & 1) * 128 + j * 32 + pp * 8;
        s16x4 lo = __builtin_amdgcn_ds_read_tr16_b64_v4i16((lds_s4p)(a0));
        s16x4 hi = __builtin_amdgcn_ds_read_tr16_b64_v4i16((lds_s4p)(a0 + 4 * 288));
        u32x2 l2 = __builtin_bit_cast(u32x2, lo), h2 = __builtin_bit_cast(u32x2, hi);
        u32 wv[4] = {l2[0], l2[1], h2[0], h2[1]};
        u32x4 o;
        o[0] = pk2(lo2f(wv[0]) * sc0[0], hi2f(wv[0]) * sc0[1]);
        o[1] = pk2(lo2f(wv[1]) * sc0[2], hi2f(wv[1]) * sc0[3]);
        o[2] = pk2(lo2f(wv[2]) * sc1[0], hi2f(wv[2]) * sc1[1]);
        o[3] = pk2(lo2f(wv[3]) * sc1[2], hi2f(wv[3]) * sc1[3]);
        float cs = 0.f, bi2 = 0.f;
#pragma unroll
        for (int e = 0; e < 4; ++e) {
          cs = dot2(o[e], 0x3f803f80u, cs);
          bi2 = dot2(wv[e], sbv[e], bi2);
        }
        st16(ws + WS_W1F + (size_t)(mtg / 7) * W1F_CB + (size_t)ks * 7168 + (mtg % 7) * 1024 + lane * 16, o);
        f32x2 r = {cs, bi2};
        *(f32x2*)(sm + K1_RED + ((w * 4 + j) * 64 + lane) * 8) = r;
      }
#pragma unroll
      for (int j = 0; j < 2; ++j) {
        const char* a0 = wst + 16 * 288 + (4 * h + qq) * 144 + j * 64 + (g & 1) * 32 + pp * 8;
        s16x4 lo = __builtin_amdgcn_ds_read_tr16_b64_v4i16((lds_s4p)(a0));
        s16x4 hi = __builtin_amdgcn_ds_read_tr16_b64_v4i16((lds_s4p)(a0 + 8 * 144));
        u32x2 l2 = __builtin_bit_cast(u32x2, lo), h2 = __builtin_bit_cast(u32x2, hi);
        u32x4 o = {l2[0], l2[1], h2[0], h2[1]};
        st16(ws + WS_WDF + ((size_t)(2 * kc + j) * 112 + 4 * slab + w) * 1024 + lane * 16, o);
      }
      __builtin_amdgcn_fence(__ATOMIC_RELEASE, "workgroup");
      u32 old = 0;
      if (lane == 0) old = __hip_atomic_fetch_add((u32*)(sm + K1_SC + 384), 1u, __ATOMIC_RELAXED, __HIP_MEMORY_SCOPE_WORKGROUP);
      old = __builtin_amdgcn_readfirstlane(old);
      if (old == 3) {
        __builtin_amdgcn_fence(__ATOMIC_ACQUIRE, "workgroup");
        const f32x2* red = (const f32x2*)(sm + K1_RED);
#pragma unroll
        for (int jj = 0; jj < 2; ++jj) {
          const int j = 2 * jj + h, r31 = lane & 31;
          f32x2 rr = {0.f, 0.f};
#pragma unroll
          for (int i = 0; i < 4; ++i) rr += red[(i * 4 + j) * 64 + r31] + red[(i * 4 + j) * 64 + 32 + r31];
          const int mtg = 4 * slab + j;
          *(f32x2*)(ws + WS_CSP + (((size_t)(mtg / 7) * 8 + kc) * 224 + (mtg % 7) * 32 + r31) * 8) = rr;
        }
      }
    }
  } else {
    const float* mod = (const float*)(ws + WS_MOD);
    const float* opc = (const float*)(ws + WS_OPC);
    u32 qp[8];
    f32x2 st[8];
#pragma unroll
    for (int i = 0; i < 8; ++i) {
      qp[i] = ((const u32*)(ws + WS_QT2))[(size_t)p * S + ci + 256 * i];
      st[i] = ((const f32x2*)(ws + WS_STATS))[ci + 256 * i];
    }
    const char* sp = ci < 16 ? ws + WS_OPC + 2048 + kc * 256 + ci * 16 : ws + WS_SBB + kc * 128 + (ci & 7) * 16;
    const u32x4 scv = *(const u32x4*)sp;
    const float sa0 = mod[2 * p], sa1 = mod[2 * p + 1];
    const float oc0 = opc[2 * p], oc1 = opc[2 * p + 1];
    const float ga0 = mod[1024 + 2 * p], ga1 = mod[1024 + 2 * p + 1];
    SCHED_FENCE();
    {
      const u32x4 z = {0, 0, 0, 0};
      if (ci < 16) *(u32x4*)(sm + K1_FC + ci * K1_CS) = z;
      if (ci >= 64 && ci < 128) *(u32x4*)(sm + K1_U + ((ci >> 5) & 1) * 4608 + 4096 + (ci & 31) * 16) = z;
      if (ci == 128) *(u32x4*)(sm + K1_SC + 384) = z;
      if (ci >= 192 && ci < 196) *(u32*)(ws + WS_FLG + (size_t)(b * 4 + ci - 192) * 64) = 0u;
      if (ci >= 200 && ci < 204) *(u32x4*)(ws + WS_ST2 + (size_t)(b * 4 + ci - 200) * 16) = z;
    }
    float h0[8], h1[8];
#pragma unroll
    for (int i = 0; i < 8; ++i) {
      u32 x = pk2(lo2f(qp[i]) - st[i][0], hi2f(qp[i]) - st[i][0]);
      x = pk2(lo2f(x) * st[i][1], hi2f(x) * st[i][1]);
      x = pk2(lo2f(x) * oc0, hi2f(x) * oc1);
      x = pk2(lo2f(x) + sa0, hi2f(x) + sa1);
      h0[i] = lo2f(x);
      h1[i] = hi2f(x);
      asm volatile("" : "+v"(h0[i]), "+v"(h1[i]));
    }
    if (ci < 24) *(u32x4*)(sm + K1_SC + ci * 16) = scv;
    STAMP(1, 1);
    RAWBAR();
    STAMP(1, 2);
    RAWBAR();
    STAMP(1, 4);
    const float* part = (const float*)(sm + K1_PART) + ((ci >> 2) & 3) * 72 + ((ci >> 4) & 15) * 4 + (ci & 3);
    const int d0 = 2 * p;
    char* dst = ws + WS_X1F + ((size_t)(d0 >> 4) * 64 + 32 * ((d0 >> 3) & 1)) * 16 + 2 * (d0 & 7) +
                (size_t)(ci >> 5) * 32768 + (ci & 31) * 16;
#pragma unroll
    for (int i = 0; i < 8; ++i) {
      float y0 = part[i * 288] + part[2304 + i * 288];
      float y1 = part[2 * 2304 + i * 288] + part[3 * 2304 + i * 288];
      u32 z = pk2(y0, y1);
      u32 a = pk2(h0[i] * lo2f(z), h1[i] * hi2f(z));
      a = pk2(ga0 * lo2f(a), ga1 * hi2f(a));
      u32 x1 = pk2(lo2f(qp[i]) + lo2f(a), hi2f(qp[i]) + hi2f(a));
      *(u32*)(dst + (size_t)i * (8 * 32768)) = x1;
    }
  }
  STAMP(1, 5);
  STAMP_FLUSH(1);
}

__global__ __launch_bounds__(512, 2) void k1_conv(const u16* __restrict__ kf, const u16* __restrict__ ssmD,
                                                  const u16* __restrict__ wg, const u16* __restrict__ wu,
                                                  const u16* __restrict__ wd, char* __restrict__ ws) {
  extern __shared__ __attribute__((aligned(16))) char sm[];
  const int w = __builtin_amdgcn_readfirstlane(threadIdx.x >> 6);
  if (w < 4) k1_body<true>(kf, ssmD, wg, wu, wd, ws, sm, w);
  else k1_body<false>(kf, ssmD, wg, wu, wd, ws, sm, w);
}

constexpr int K2_G = 28672;
constexpr int K2_NS = 4;
constexpr int K2_COR = K2_NS * K2_G;
constexpr int K2_FREE = K2_COR + 8192;
constexpr int K2_LDS = 163840;

#define GLOAD(dst, voff, sbase, imm) \
  asm volatile("global_load_dwordx4 %0, %1, %2 offset:" #imm : "=v"(dst) : "v"(voff), "s"(sbase) : "memory")
#define GDMA(voff, sbase, ldsa, imm)                                                               \
  asm volatile("s_mov_b32 m0, %2\n\ts_nop 0\n\tglobal_load_lds_dwordx4 %0, %1 offset:" #imm       \
               :: "v"(voff), "s"(sbase), "s"(ldsa) : "memory")

DI constexpr int wd_lds_off(int q, int w) {
  return q < 10 ? K2_FREE + w * 10240 + q * 1024
       : q < 17 ? 0 * K2_G + w * 7168 + (q - 10) * 1024
       : q < 24 ? 1 * K2_G + w * 7168 + (q - 17) * 1024
                : 2 * K2_G + w * 7168 + (q - 24) * 1024;
}

__global__ __launch_bounds__(256, 1) __attribute__((amdgpu_waves_per_eu(1, 1))) void k23_mlp(char* __restrict__ ws,
                                                                                             u16* __restrict__ out) {
  extern __shared__ __attribute__((aligned(16))) char sm[];
  const int b = blockIdx.x, t = threadIdx.x;
  STAMP_DECL
  STAMP(2, 0);
  const int w = __builtin_amdgcn_readfirstlane(t >> 6), lane = t & 63;
  const int xcd = b & 7, idx = b >> 3;
  const int rbk = (xcd & 1) * 8 + (idx & 7);
  const int cbk = (xcd >> 1) * 4 + (idx >> 3);
  const int st = rbk * 4 + w;
  const int sbk = xcd * 4 + (idx & 3);
  const int nbk = idx >> 2;
  const char* wsrc = ws + WS_W1F + (size_t)cbk * W1F_CB + w * 7168;
  const char* asrc = ws + WS_X1F + (size_t)st * 32768;
  const char* dsrc = ws + WS_WDF + ((size_t)(2 * nbk) * 112 + 28 * w) * 1024;
  const unsigned lbase = (unsigned)(uintptr_t)(__attribute__((address_space(3))) char*)sm;
  const unsigned ldst = lbase + w * 7168;
  const unsigned voff = lane * 16;

  u32x4 act[32];
#define VM(g, j)                                                                                   \
  {                                                                                                \
    if ((j) == 0) GLOAD(act[4 * (g) + 0], voff, asrc + (g) * 4096, 0);                             \
    if ((j) == 1) GLOAD(act[4 * (g) + 1], voff, asrc + (g) * 4096, 1024);                          \
    if ((j) == 2) GLOAD(act[4 * (g) + 2], voff, asrc + (g) * 4096, 2048);                          \
    if ((j) == 3) GLOAD(act[4 * (g) + 3], voff, asrc + (g) * 4096, 3072);                          \
    if ((j) == 4) GDMA(voff, wsrc + (g) * K2_G, ldst + ((g) % K2_NS) * K2_G, 0);                   \
    if ((j) == 5) GDMA(voff, wsrc + (g) * K2_G, ldst + ((g) % K2_NS) * K2_G, 1024);                \
    if ((j) == 6) GDMA(voff, wsrc + (g) * K2_G, ldst + ((g) % K2_NS) * K2_G, 2048);                \
    if ((j) == 7) GDMA(voff, wsrc + (g) * K2_G, ldst + ((g) % K2_NS) * K2_G, 3072);                \
    if ((j) == 8) GDMA(voff, wsrc + (g) * K2_G + 4096, ldst + ((g) % K2_NS) * K2_G + 4096, 0);     \
    if ((j) == 9) GDMA(voff, wsrc + (g) * K2_G + 4096, ldst + ((g) % K2_NS) * K2_G + 4096, 1024);  \
    if ((j) == 10) GDMA(voff, wsrc + (g) * K2_G + 4096, ldst + ((g) % K2_NS) * K2_G + 4096, 2048); \
  }
#define VMALL(g) { VM(g,4) VM(g,0) VM(g,5) VM(g,6) VM(g,7) VM(g,8) VM(g,9) VM(g,10) VM(g,1) VM(g,2) VM(g,3) }
#define WDM(q) { if ((q) >= 0 && (q) < 30) GDMA(voff, dsrc + ((q) & 1) * (112 * 1024) + ((q) >> 1) * 1024, lbase + wd_lds_off((q) < 0 ? 0 : (q), w), 0); }
  u32x4 sx[2];
  u32x2 cp[8];
  {
    const char* sbase = ws + WS_X1F + (size_t)(rbk * 4 + (cbk >> 2)) * 32768 + (size_t)w * 8192 + (cbk & 3) * 128;
    const unsigned so = (lane >> 3) * 512 + (lane & 7) * 16;
    GLOAD(sx[0], so, sbase, 0);
    GLOAD(sx[1], so, sbase + 4096, 0);
    const char* cbase = ws + WS_CSP + (size_t)cbk * (8 * 224 * 8);
    const unsigned co = (t < 224 ? t : 0) * 8;
#pragma unroll
    for (int i = 0; i < 8; ++i)
      asm volatile("global_load_dwordx2 %0, %1, %2" : "=v"(cp[i]) : "v"(co), "s"(cbase + i * 1792) : "memory");
  }
  VMALL(0) VMALL(1) VMALL(2)

  f32x16 acc[7];
#pragma unroll
  for (int m = 0; m < 7; ++m)
#pragma unroll
    for (int r = 0; r < 16; ++r) acc[m][r] = 0.f;
  const char* lsrc = sm + lane * 16;
  bf16x8 wf[2][7];

#define WAITG(g, n)                                                                                           \
  SCHED_FENCE();                                                                                              \
  asm volatile("s_waitcnt vmcnt(" #n ") lgkmcnt(0)"                                                           \
               : "+v"(act[4 * (g)]), "+v"(act[4 * (g) + 1]), "+v"(act[4 * (g) + 2]), "+v"(act[4 * (g) + 3]) \
               :                                                                                              \
               : "memory");                                                                                   \
  __builtin_amdgcn_s_barrier();                                                                               \
  asm volatile("" ::: "memory");                                                                              \
  SCHED_FENCE();
#define KOFF(i) ((i) == 32 ? K2_COR : (((i) >> 2) % K2_NS) * K2_G + ((i) & 3) * 7168)
#define RD(i, m) wf[(i) & 1][m] = *(const bf16x8*)(lsrc + KOFF(i) + (m) * 1024);
#define MM(i, m) acc[m] = __builtin_amdgcn_mfma_f32_32x32x16_bf16(wf[(i) & 1][m], __builtin_bit_cast(bf16x8, act[i]), acc[m], 0, 0, 0);
#define VX(vg, j0, q0, k) { if ((vg) >= 0) VM((vg) < 0 ? 0 : (vg), (j0) + (k)) else WDM((q0) < 0 ? -1 : (q0) + (k)) }
  u32x4 wr2[26];
#define WDR(r) { if ((r) >= 0 && (r) < 26) GLOAD(wr2[(r) < 0 ? 0 : (r) % 26], voff, dsrc + ((r) & 1) * (112 * 1024) + (15 + ((r) >> 1)) * 1024, 0); }
#define STEPR(i, vg, j0, q0, r0)                                                 \
  {                                                                              \
    MM(i, 0) RD((i) + 1, 0) RD((i) + 1, 1) SCHED_FENCE();                        \
    MM(i, 1) RD((i) + 1, 2) RD((i) + 1, 3) WDR(r0) SCHED_FENCE();                \
    MM(i, 2) RD((i) + 1, 4) RD((i) + 1, 5) WDR((r0) < 0 ? -1 : (r0) + 1) SCHED_FENCE(); \
    MM(i, 3) RD((i) + 1, 6) WDR((r0) < 0 ? -1 : (r0) + 2) SCHED_FENCE();         \
    MM(i, 4) VX(vg, j0, q0, 0) SCHED_FENCE();                                    \
    MM(i, 5) VX(vg, j0, q0, 1) SCHED_FENCE();                                    \
    MM(i, 6) VX(vg, j0, q0, 2) SCHED_FENCE();                                    \
  }
#define STEP(i, vg, j0, q0) STEPR(i, vg, j0, q0, -1)
  SCHED_FENCE();
  asm volatile("s_waitcnt vmcnt(33)"
               : "+v"(sx[0]), "+v"(sx[1]), "+v"(cp[0]), "+v"(cp[1]), "+v"(cp[2]), "+v"(cp[3]), "+v"(cp[4]), "+v"(cp[5]),
                 "+v"(cp[6]), "+v"(cp[7])
               :
               : "memory");
  {
    float sum = 0.f, sq = 0.f;
#pragma unroll
    for (int i = 0; i < 2; ++i)
#pragma unroll
      for (int j = 0; j < 4; ++j) {
        sum = dot2(sx[i][j], 0x3f803f80u, sum);
        sq = dot2(sx[i][j], sx[i][j], sq);
      }
    sum = red_8_16_32(sum);
    sq = red_8_16_32(sq);
    if (lane < 8) *(f32x2*)(sm + K2_COR + 7168 + (w * 8 + lane) * 8) = (f32x2){sum, sq};
    float cs = 0.f, bs = 0.f;
#pragma unroll
    for (int i = 0; i < 8; ++i) {
      cs += __uint_as_float(cp[i][0]);
      bs += __uint_as_float(cp[i][1]);
    }
    const float c0 = rb(cs), c1 = rb(cs - c0), c2 = rb(cs - c0 - c1);
    const float b0 = rb(bs), b1 = rb(bs - b0), b2 = rb(bs - b0 - b1);
    if (t < 224) {
      char* cd = sm + K2_COR + (t >> 5) * 1024 + (t & 31) * 16;
      *(u32x4*)cd = (u32x4){pk2(c0, c1), pk2(c2, b0), pk2(b1, b2), 0u};
      *(u32x4*)(cd + 512) = (u32x4){0u, 0u, 0u, 0u};
    }
  }
  WAITG(0, 22)
  STAMP(2, 1);
  RD(0, 0) RD(0, 1) RD(0, 2) RD(0, 3) RD(0, 4) RD(0, 5) RD(0, 6)
  if (w == 0) {
    if (lane < 8) {
      const f32x2* ps = (const f32x2*)(sm + K2_COR + 7168);
      const f32x2 r = ps[lane] + ps[8 + lane] + ps[16 + lane] + ps[24 + lane];
      const float mean = r[0] * (1.f / D);
      const float var = fmaxf(r[1] * (1.f / D) - mean * mean, 0.f);
      const float sdv = __builtin_amdgcn_sqrtf(rb(rb(var) + 1e-5f));
      const unsigned long long gr = (1ull << 32) | pk2(mean, sdv);
      __hip_atomic_store((unsigned long long*)(ws + WS_ST2) + rbk * 128 + cbk * 8 + lane, gr, __ATOMIC_RELAXED,
                         __HIP_MEMORY_SCOPE_AGENT);
    }
  }
  VM(3, 0) VM(3, 1)
  SCHED_FENCE();
  STEP(0, 3, 2, -1) STEP(1, 3, 5, -1) STEP(2, 3, 8, -1)
  WAITG(1, 22)
  STEP(3, 4, 0, -1) STEP(4, 4, 3, -1) STEP(5, 4, 6, -1) STEP(6, 4, 9, -1)
  WAITG(2, 22)
  STEP(7, 5, 0, -1) STEP(8, 5, 3, -1) STEP(9, 5, 6, -1) STEP(10, 5, 9, -1)
  WAITG(3, 22)
  STEP(11, 6, 0, -1) STEP(12, 6, 3, -1) STEP(13, 6, 6, -1) STEP(14, 6, 9, -1)
  WAITG(4, 22)
  STEP(15, 7, 0, -1) STEP(16, 7, 3, -1) STEP(17, 7, 6, -1) STEP(18, 7, 9, -1)
  u32x2 stg;
  const char* stp = ws + WS_ST2 + (size_t)st * 256;
  const unsigned sto = (lane & 31) * 8;
  asm volatile("global_load_dwordx2 %0, %1, %2 sc1" : "=v"(stg) : "v"(sto), "s"(stp) : "memory");
  WAITG(5, 23)
  STEP(19, -1, 0, 0) STEP(20, -1, 0, 3) STEP(21, -1, 0, 6) STEP(22, -1, 0, 9)
  WAITG(6, 24)
  STEP(23, -1, 0, 12) STEP(24, -1, 0, 15) STEP(25, -1, 0, 18) STEP(26, -1, 0, 21)
  WAITG(7, 24)
  asm volatile("" : "+v"(stg));
  for (int spins = 0; !__all(stg[1] == 1u) && spins < 20000; ++spins) {
    __builtin_amdgcn_s_sleep(1);
    asm volatile("global_load_dwordx2 %0, %1, %2 sc1\n\ts_waitcnt vmcnt(0)" : "=&v"(stg) : "v"(sto), "s"(stp) : "memory");
  }
  SCHED_FENCE();
  STEP(27, -1, 0, 24)
  bf16x8 tl[2][4];
#define RDT(m, k) tl[(m) & 1][k] = *(const bf16x8*)(lsrc + KOFF(29 + (k)) + (m) * 1024);
  const float mb = lo2f(stg[0]), sd = hi2f(stg[0]);
  float rs2;
  {
    MM(28, 0) RDT(0, 0) RDT(0, 1) SCHED_FENCE();
    MM(28, 1) RDT(0, 2) RDT(0, 3) SCHED_FENCE();
    MM(28, 2) WDM(27) SCHED_FENCE();
    MM(28, 3) WDM(28) SCHED_FENCE();
    MM(28, 4) WDM(29) SCHED_FENCE();
    MM(28, 5)
    const float rs = __builtin_amdgcn_rcpf(sd);
    rs2 = rs * rs;
    SCHED_FENCE();
    MM(28, 6) SCHED_FENCE();
  }
  STAMP(2, 2);
  const u32x4 cb4 = {pk2(-mb, -mb), pk2(-mb, sd), pk2(sd, sd), 0u};
  const auto wsr = __builtin_amdgcn_make_buffer_rsrc(ws, 0, 0x7fffffff, 0x00020000);
  const unsigned hoff = (unsigned)WS_HHF + ((unsigned)st * 112 + cbk * 7) * 1024;
  u32x4 eo;
#define EPJ(m, j)                                                                     \
  {                                                                                   \
    float u0 = acc[m][8 + 2 * (j)], u1 = acc[m][8 + 2 * (j) + 1];                     \
    asm volatile("" : "+v"(u0), "+v"(u1));                                            \
    float e0 = relu(acc[m][2 * (j)]) * u0 * rs2;                                      \
    float e1 = relu(acc[m][2 * (j) + 1]) * u1 * rs2;                                  \
    eo[j] = pk2(e0, e1);                                                              \
  }
#define EST(m) __builtin_amdgcn_raw_buffer_store_b128(eo, wsr, voff + (m) * 1024, hoff, 16);
#define MT(m, k, bop) acc[m] = __builtin_amdgcn_mfma_f32_32x32x16_bf16(tl[(m) & 1][k], __builtin_bit_cast(bf16x8, bop), acc[m], 0, 0, 0);
#define TAIL(m)                                                                                      \
  {                                                                                                  \
    MT(m, 0, act[29]) SCHED_FENCE();                                                                 \
    if ((m) < 6) { RDT((m) + 1, 0) RDT((m) + 1, 1) }                                                 \
    SCHED_FENCE();                                                                                   \
    MT(m, 1, act[30]) SCHED_FENCE();                                                                 \
    if ((m) < 6) { RDT((m) + 1, 2) RDT((m) + 1, 3) }                                                 \
    if ((m) > 0) EPJ((m) > 0 ? (m) - 1 : 0, 0)                                                       \
    SCHED_FENCE();                                                                                   \
    MT(m, 2, act[31]) SCHED_FENCE();                                                                 \
    if ((m) > 0) { EPJ((m) > 0 ? (m) - 1 : 0, 1) EPJ((m) > 0 ? (m) - 1 : 0, 2) }                     \
    SCHED_FENCE();                                                                                   \
    MT(m, 3, cb4) SCHED_FENCE();                                                                     \
    if ((m) > 0) { EPJ((m) > 0 ? (m) - 1 : 0, 3) EST((m) > 0 ? (m) - 1 : 0) }                        \
    SCHED_FENCE();                                                                                   \
  }
  TAIL(0) TAIL(1) TAIL(2) TAIL(3) TAIL(4) TAIL(5) TAIL(6)
  EPJ(6, 0) EPJ(6, 1) EPJ(6, 2) EPJ(6, 3) EST(6)
  asm volatile("s_waitcnt vmcnt(0)" ::: "memory");
  u32* flg = (u32*)(ws + WS_FLG);
  if (lane == 0) __hip_atomic_store(flg + (st * 16 + cbk) * 16, 1u, __ATOMIC_RELAXED, __HIP_MEMORY_SCOPE_AGENT);
  STAMP(2, 3);
  SCHED_FENCE();

#pragma unroll
  for (int r = 0; r < 26; ++r) wr2[r] = *(const u32x4*)(dsrc + (r & 1) * (112 * 1024) + (15 + (r >> 1)) * 1024 + voff);
  const int fn = w >> 1, fs = w & 1;
  const int h = lane >> 5;
  const int ncol = 64 * nbk + 32 * fn;
  u32x4 xr[2];
  f32x4 gv[4];
  {
    const char* xsrc = ws + WS_X1F + ((size_t)(2 * sbk + fs) * 32 + (ncol >> 4)) * 1024 + lane * 16;
    xr[0] = *(const u32x4*)(xsrc);
    xr[1] = *(const u32x4*)(xsrc + 1024);
    const float* gbp = (const float*)(ws + WS_MOD) + 5 * D + ncol + 8 * h;
    gv[0] = *(const f32x4*)(gbp);
    gv[1] = *(const f32x4*)(gbp + 4);
    gv[2] = *(const f32x4*)(gbp + 16);
    gv[3] = *(const f32x4*)(gbp + 20);
  }
  SCHED_FENCE();
  {
    const u32* fp = flg + ((2 * sbk + (lane & 1)) * 16 + 4 * w + ((lane >> 1) & 3)) * 16;
    for (int spins = 0; spins < 20000; ++spins) {
      u32 f = __hip_atomic_load(fp, __ATOMIC_RELAXED, __HIP_MEMORY_SCOPE_AGENT);
      if (__all(f == 1u)) break;
      __builtin_amdgcn_s_sleep(1);
    }
    __builtin_amdgcn_fence(__ATOMIC_ACQUIRE, "wavefront");
  }
  STAMP(2, 4);
  SCHED_FENCE();
  const unsigned hs = (unsigned)WS_HHF + ((unsigned)(2 * sbk) * 112 + 28 * w) * 1024;
  constexpr int WIN = 16;
  u32x4 hb[WIN][2];
#define HLD(i)                                                                                          \
  {                                                                                                     \
    hb[(i) % WIN][0] = __builtin_amdgcn_raw_buffer_load_b128(wsr, voff, hs + (i) * 1024, 16);           \
    hb[(i) % WIN][1] = __builtin_amdgcn_raw_buffer_load_b128(wsr, voff, hs + (112 + (i)) * 1024, 16);   \
  }
#pragma unroll
  for (int i = 0; i < WIN; ++i) HLD(i)
  SCHED_FENCE();
  f32x16 ac2[2][2];
#pragma unroll
  for (int a = 0; a < 2; ++a)
#pragma unroll
    for (int c = 0; c < 2; ++c)
#pragma unroll
      for (int r = 0; r < 16; ++r) ac2[a][c][r] = 0.f;
  bf16x8 wl2[2][2];
  wl2[0][0] = *(const bf16x8*)(lsrc + wd_lds_off(0, w));
  wl2[0][1] = *(const bf16x8*)(lsrc + wd_lds_off(1, w));
#pragma unroll
  for (int i = 0; i < 28; ++i) {
    if (i + 1 < 15) {
      wl2[(i + 1) & 1][0] = *(const bf16x8*)(lsrc + wd_lds_off(2 * (i + 1), w));
      wl2[(i + 1) & 1][1] = *(const bf16x8*)(lsrc + wd_lds_off(2 * (i + 1) + 1, w));
    }
#pragma unroll
    for (int a = 0; a < 2; ++a)
#pragma unroll
      for (int c = 0; c < 2; ++c)
        ac2[a][c] = __builtin_amdgcn_mfma_f32_32x32x16_bf16(i < 15 ? wl2[i & 1][a] : __builtin_bit_cast(bf16x8, wr2[i < 15 ? 0 : 2 * (i - 15) + a]),
                                                            __builtin_bit_cast(bf16x8, hb[i % WIN][c]), ac2[a][c], 0, 0, 0);
    if (i == 0) STAMP(2, 5);
    if (i + WIN < 28) HLD(i + WIN)
    SCHED_FENCE();
  }
  STAMP(2, 6);
#define PARK(w_, j) ((j) == 1 ? K2_FREE + (w_) * 10240 : (j) == 2 ? K2_FREE + (w_) * 10240 + 4096 : (w_) * 7168)
#pragma unroll
  for (int a = 0; a < 2; ++a)
#pragma unroll
    for (int c = 0; c < 2; ++c) {
      const int tile = a * 2 + c;
      if (tile != w) {
        const int j = (tile - w) & 3;
        char* dst = sm + PARK(w, j) + lane * 16;
#pragma unroll
        for (int r = 0; r < 4; ++r)
          *(f32x4*)(dst + r * 1024) =
              (f32x4){ac2[a][c][4 * r], ac2[a][c][4 * r + 1], ac2[a][c][4 * r + 2], ac2[a][c][4 * r + 3]};
      }
    }
  __syncthreads();
  f32x16 fin;
  if (w == 0) fin = ac2[0][0];
  else if (w == 1) fin = ac2[0][1];
  else if (w == 2) fin = ac2[1][0];
  else fin = ac2[1][1];
#pragma unroll
  for (int o = 1; o < 4; ++o) {
    const int ow = (w + o) & 3;
    const char* src = sm + PARK(ow, 4 - o) + lane * 16;
#pragma unroll
    for (int r = 0; r < 4; ++r) {
      f32x4 v = *(const f32x4*)(src + r * 1024);
#pragma unroll
      for (int e = 0; e < 4; ++e) fin[4 * r + e] += v[e];
    }
  }
  u32 pkd[8];
#pragma unroll
  for (int j = 0; j < 8; ++j) pkd[j] = pk2(fin[2 * j], fin[2 * j + 1]);
  const int srow = 64 * sbk + 32 * fs + (lane & 31);
  u16* orow = out + (size_t)srow * D + ncol + 8 * h;
#pragma unroll
  for (int kk = 0; kk < 2; ++kk) {
    auto r0 = __builtin_amdgcn_permlane32_swap(pkd[4 * kk], pkd[4 * kk + 2], false, false);
    auto r1 = __builtin_amdgcn_permlane32_swap(pkd[4 * kk + 1], pkd[4 * kk + 3], false, false);
    u32 m4[4] = {r0[0], r1[0], r0[1], r1[1]};
    u32x4 o;
#pragma unroll
    for (int j = 0; j < 4; ++j) {
      float g0 = gv[2 * kk + (j >> 1)][(j & 1) * 2], g1 = gv[2 * kk + (j >> 1)][(j & 1) * 2 + 1];
      u32 a = pk2(g0 * lo2f(m4[j]), g1 * hi2f(m4[j]));
      o[j] = pk2(lo2f(xr[kk][j]) + lo2f(a), hi2f(xr[kk][j]) + hi2f(a));
    }
    st16(orow + 16 * kk, o);
  }
  STAMP(2, 7);
  STAMP_FLUSH(2);
}

extern "C" void kernel_launch(void* const* d_in, const int* in_sizes, int n_in, void* d_out, int out_size,
                              void* d_ws, size_t ws_size, hipStream_t stream) {
  const u16* q = (const u16*)d_in[0];
  const u16* k = (const u16*)d_in[1];
  const u16* v = (const u16*)d_in[2];
  const u16* shk = (const u16*)d_in[3];
  const u16* kf = (const u16*)d_in[4];
  const u16* sD = (const u16*)d_in[5];
  const u16* wg = (const u16*)d_in[7];
  const u16* wu = (const u16*)d_in[8];
  const u16* wd = (const u16*)d_in[9];
  const u16* c = (const u16*)d_in[10];
  const u16* wm = (const u16*)d_in[11];
  char* ws = (char*)d_ws;
  (void)hipFuncSetAttribute((const void*)k1_conv, hipFuncAttributeMaxDynamicSharedMemorySize, K1_LDS);
  (void)hipFuncSetAttribute((const void*)k23_mlp, hipFuncAttributeMaxDynamicSharedMemorySize, K2_LDS);
  k0_prep<<<K0_NA + K0_NB + K0_NC, 256, 0, stream>>>(q, k, v, shk, c, wm, ws);
  k1_conv<<<256, 512, K1_LDS, stream>>>(kf, sD, wg, wu, wd, ws);
  k23_mlp<<<256, 256, K2_LDS, stream>>>(ws, (u16*)d_out);
}
```

```cpp
#define PF_DATA 0
#define PF_CODE 1
#include <hip/hip_runtime.h>
#include <stdint.h>

typedef unsigned short u16;
typedef unsigned int u32;
typedef __attribute__((ext_vector_type(8))) short bf16x8;
typedef __attribute__((ext_vector_type(4))) short s16x4;
typedef __attribute__((ext_vector_type(4))) float f32x4;
typedef __attribute__((ext_vector_type(2))) float f32x2;
typedef __attribute__((ext_vector_type(16))) float f32x16;
typedef __attribute__((ext_vector_type(4))) unsigned u32x4;
typedef __attribute__((ext_vector_type(2))) unsigned u32x2;
typedef __attribute__((ext_vector_type(2))) __bf16 bf2;
typedef __attribute__((address_space(3))) s16x4* lds_s4p;

#define DI __device__ __forceinline__

constexpr int S = 2048, D = 512, F = 1792;

DI float lo2f(u32 x) { return __uint_as_float(x << 16); }
DI float hi2f(u32 x) { return __uint_as_float(x & 0xffff0000u); }
DI u32 pk2(float a, float b) {
  f32x2 v = {a, b};
  return __builtin_bit_cast(u32, __builtin_convertvector(v, bf2));
}
DI float rb(float a) { return lo2f(pk2(a, a)); }
DI float dot2(u32 a, u32 b, float c) {
  return __builtin_amdgcn_fdot2_f32_bf16(__builtin_bit_cast(bf2, a), __builtin_bit_cast(bf2, b), c, false);
}

DI float red_8_16_32(float x) {
  x += __uint_as_float(__builtin_amdgcn_update_dpp(0u, __float_as_uint(x), 0x128, 0xf, 0xf, false));
  auto a = __builtin_amdgcn_permlane16_swap(__float_as_uint(x), __float_as_uint(x), false, false);
  x = __uint_as_float(a[0]) + __uint_as_float(a[1]);
  auto b = __builtin_amdgcn_permlane32_swap(__float_as_uint(x), __float_as_uint(x), false, false);
  return __uint_as_float(b[0]) + __uint_as_float(b[1]);
}
DI float relu(float x) {
  float r;
  asm("v_max_f32 %0, 0, %1" : "=v"(r) : "v"(x));
  return r;
}

#define PFL(d, ptr) asm volatile("global_load_dwordx4 %0, %1, off" : "=v"(d) : "v"(ptr) : "memory")
#define STMODE 0
DI void st16(void* p, u32x4 v) {
  *(u32x4*)p = v;
}

#define STAMP_DECL
#define STAMP(kid, i)
#define STAMP_FLUSH(kid)
#define SCHED_FENCE() __builtin_amdgcn_sched_barrier(0)
#define RAWBAR()                                          \
  do {                                                    \
    __builtin_amdgcn_sched_barrier(0);                    \
    asm volatile("s_waitcnt lgkmcnt(0)" ::: "memory");    \
    __builtin_amdgcn_s_barrier();                         \
    asm volatile("" ::: "memory");                        \
    __builtin_amdgcn_sched_barrier(0);                    \
  } while (0)
#define SGB(mask, n) __builtin_amdgcn_sched_group_barrier(mask, n, 0)

constexpr size_t WS_MOD = 0;
constexpr size_t WS_OPC = WS_MOD + 3072 * 4;
constexpr size_t WS_SBB = WS_OPC + 1024 * 4;
constexpr size_t WS_STATS = 32768;
constexpr size_t WS_URT = 65536;
constexpr size_t WS_QT2 = WS_URT + 2u * S * D;
constexpr size_t WS_X1F = WS_QT2 + 2u * S * D;
constexpr size_t WS_W1F = WS_X1F + 2u * S * D;
constexpr size_t W1F_CB = 32 * 7 * 1024;
constexpr size_t WS_WDF = WS_W1F + 16 * W1F_CB;
constexpr size_t WS_HHF = WS_WDF + 2u * F * D;
constexpr size_t WS_FLG = WS_HHF + 2u * S * F;
constexpr size_t WS_CSP = WS_FLG + 1024 * 64;
constexpr size_t WS_ST2 = WS_CSP + 16 * 8 * 224 * 8;
constexpr size_t WS_END = WS_ST2 + 2048 * 8;

constexpr int K0_NA = 256, K0_NB = 128, K0_NC = 96;

__global__ __launch_bounds__(256) void k0_prep(const u16* __restrict__ q, const u16* __restrict__ k,
                                               const u16* __restrict__ v, const u16* __restrict__ shk,
                                               const u16* __restrict__ cc, const u16* __restrict__ wmod,
                                               const u16* __restrict__ kf, const u16* __restrict__ wg,
                                               const u16* __restrict__ wu, const u16* __restrict__ wd,
                                               char* __restrict__ ws) {
  __shared__ __attribute__((aligned(16))) u32 sm[4096];
  const int t = threadIdx.x;
  const int b0 = blockIdx.x;
  const int b = b0 < K0_NC ? b0 + K0_NA + K0_NB : b0 < K0_NC + K0_NB ? b0 - K0_NC + K0_NA : b0 - K0_NC - K0_NB;
  STAMP_DECL
  STAMP(0, 0);
  if (b < K0_NA) {
    const int sb = b >> 3, db = b & 7;
    const int rp = t >> 3, ci = t & 7;
    const int s0 = 64 * sb + 2 * rp, ch0 = 64 * db + 8 * ci;
    u32x4 kr[5], vr[2], qr[2], wr[4];
#pragma unroll
    for (int i = 0; i < 5; ++i) {
      int s = s0 - 3 + i;
      kr[i] = *(const u32x4*)(k + (size_t)(s < 0 ? 0 : s) * D + ch0);
    }
#pragma unroll
    for (int i = 0; i < 2; ++i) {
      vr[i] = *(const u32x4*)(v + (size_t)(s0 + i) * D + ch0);
      qr[i] = *(const u32x4*)(q + (size_t)(s0 + i) * D + ch0);
    }
#pragma unroll
    for (int i = 0; i < 4; ++i) wr[i] = *(const u32x4*)(shk + (size_t)ch0 * 4 + i * 8);
    SCHED_FENCE();
#pragma unroll
    for (int i = 0; i < 3; ++i) {
      const bool neg = (s0 - 3 + i) < 0;
#pragma unroll
      for (int j = 0; j < 4; ++j) kr[i][j] = neg ? 0u : kr[i][j];
    }
    u32* Tu = sm;
    u32* Tq = sm + 2048;
    const int x = 31 - rp;
#pragma unroll
    for (int j = 0; j < 4; ++j) {
      int pr = 4 * ci + j;
      int xs = 2 * rp;
      u32x2 val = {qr[0][j], qr[1][j]};
      *(u32x2*)&Tq[pr * 64 + ((((xs >> 2) ^ (2 * ci)) << 2) | (xs & 3))] = val;
    }
#pragma unroll
    for (int j = 0; j < 8; ++j) {
      u32 w01 = wr[j >> 1][(j & 1) * 2], w23 = wr[j >> 1][(j & 1) * 2 + 1];
      float w0 = lo2f(w01), w1 = hi2f(w01), w2 = lo2f(w23), w3 = hi2f(w23);
      float kk[5];
#pragma unroll
      for (int i = 0; i < 5; ++i) kk[i] = (j & 1) ? hi2f(kr[i][j >> 1]) : lo2f(kr[i][j >> 1]);
      u32 p = pk2(kk[0] * w3, kk[1] * w3);
      float a0 = lo2f(p), a1 = hi2f(p);
      p = pk2(kk[1] * w2, kk[2] * w2);
      p = pk2(a0 + lo2f(p), a1 + hi2f(p));
      a0 = lo2f(p); a1 = hi2f(p);
      p = pk2(kk[2] * w1, kk[3] * w1);
      p = pk2(a0 + lo2f(p), a1 + hi2f(p));
      a0 = lo2f(p); a1 = hi2f(p);
      p = pk2(kk[3] * w0, kk[4] * w0);
      p = pk2(a0 + lo2f(p), a1 + hi2f(p));
      a0 = lo2f(p); a1 = hi2f(p);
      float v0 = (j & 1) ? hi2f(vr[0][j >> 1]) : lo2f(vr[0][j >> 1]);
      float v1 = (j & 1) ? hi2f(vr[1][j >> 1]) : lo2f(vr[1][j >> 1]);
      u32 u = pk2(a1 * v1, a0 * v0);
      int ch = 8 * ci + j;
      Tu[ch * 32 + ((((x >> 2) ^ ci) << 2) | (x & 3))] = u;
    }
    STAMP(0, 1);
    __syncthreads();
    u16* urT = (u16*)(ws + WS_URT);
    u32* qT2 = (u32*)(ws + WS_QT2);
    const int tbase = S - 64 * (sb + 1);
#pragma unroll
    for (int i = 0; i < 2; ++i) {
      int idx = t + 256 * i;
      int ch = idx >> 3, c8 = idx & 7;
      u32x4 val = *(const u32x4*)&Tu[ch * 32 + ((c8 ^ (ch >> 3)) << 2)];
      st16(urT + (size_t)(64 * db + ch) * S + tbase + c8 * 8, val);
      int pr = idx >> 4, c16 = idx & 15;
      u32x4 qv = *(const u32x4*)&Tq[pr * 64 + ((c16 ^ (2 * (pr >> 2))) << 2)];
      st16(qT2 + (size_t)(32 * db + pr) * S + 64 * sb + c16 * 4, qv);
    }
  } else if (b < K0_NA + K0_NB) {
    const int w = t >> 6, lane = t & 63;
    const int r0 = (b - K0_NA) * 16 + w * 4;
    u32x4 xr[4];
#pragma unroll
    for (int i = 0; i < 4; ++i) xr[i] = *(const u32x4*)(q + (size_t)(r0 + i) * D + lane * 8);
    SCHED_FENCE();
    u32x4 cpf;
    {
      unsigned long long c0, c1;
      int r0_, r1_;
      asm volatile("s_getpc_b64 %0\n\ts_mov_b32 %1, _Z7k1_convPKtS0_S0_S0_S0_Pc@rel32@lo+4" : "=s"(c0), "=s"(r0_));
      asm volatile("s_getpc_b64 %0\n\ts_mov_b32 %1, _Z5k_endPc@rel32@lo+4" : "=s"(c1), "=s"(r1_));
      c0 += (long long)r0_;
      c1 += (long long)r1_;
      const unsigned nbytes = (unsigned)(c1 - c0);
      const unsigned off = (((b - K0_NA) >> 3) * 256 + t) * 16;
      PFL(cpf, (const char*)c0 + (off + 16 <= nbytes ? off : 0u));
    }
    SCHED_FENCE();
    float sum[4];
#pragma unroll
    for (int i = 0; i < 4; ++i) {
      float a = 0.f;
#pragma unroll
      for (int j = 0; j < 4; ++j) a += lo2f(xr[i][j]) + hi2f(xr[i][j]);
      sum[i] = a;
    }
    STAMP(0, 1);
#pragma unroll
    for (int o = 32; o; o >>= 1)
#pragma unroll
      for (int i = 0; i < 4; ++i) sum[i] += __shfl_xor(sum[i], o);
    float sq[4];
#pragma unroll
    for (int i = 0; i < 4; ++i) {
      float m = sum[i] * (1.f / D), a = 0.f;
#pragma unroll
      for (int j = 0; j < 4; ++j) {
        float d0 = lo2f(xr[i][j]) - m, d1 = hi2f(xr[i][j]) - m;
        a += d0 * d0 + d1 * d1;
      }
      sq[i] = a;
    }
#pragma unroll
    for (int o = 32; o; o >>= 1)
#pragma unroll
      for (int i = 0; i < 4; ++i) sq[i] += __shfl_xor(sq[i], o);
    if (lane < 4) {
      float sm_ = lane == 0 ? sum[0] : lane == 1 ? sum[1] : lane == 2 ? sum[2] : sum[3];
      float sq_ = lane == 0 ? sq[0] : lane == 1 ? sq[1] : lane == 2 ? sq[2] : sq[3];
      float mb = rb(sm_ * (1.f / D));
      float sd = rb(sqrtf(rb(rb(sq_ * (1.f / D)) + 1e-5f)));
      f32x2 o = {mb, 1.0f / sd};
      ((f32x2*)(ws + WS_STATS))[r0 + lane] = o;
    }
    asm volatile("s_waitcnt vmcnt(0)" :: "v"(cpf) : "memory");
  } else {
    const int cbi = b - K0_NA - K0_NB;
    const int cb = (cbi & 7) * 12 + (cbi >> 3);
    const int kq = t >> 2, cq = t & 3;
    u32x4 wv[8];
    u32x4 cv = *(const u32x4*)(cc + 8 * kq);
#pragma unroll
    for (int i = 0; i < 8; ++i) wv[i] = *(const u32x4*)(wmod + (size_t)(8 * kq + i) * (6 * D) + 32 * cb + 8 * cq);
    SCHED_FENCE();
    float acc[8];
#pragma unroll
    for (int j = 0; j < 8; ++j) acc[j] = 0.f;
#pragma unroll
    for (int i = 0; i < 8; ++i) {
      float x = (i & 1) ? hi2f(cv[i >> 1]) : lo2f(cv[i >> 1]);
      float sl = rb(x / (1.f + expf(-x)));
#pragma unroll
      for (int j = 0; j < 4; ++j) {
        acc[2 * j] += sl * lo2f(wv[i][j]);
        acc[2 * j + 1] += sl * hi2f(wv[i][j]);
      }
    }
    STAMP(0, 1);
#pragma unroll
    for (int o = 4; o < 64; o <<= 1)
#pragma unroll
      for (int j = 0; j < 8; ++j) acc[j] += __shfl_xor(acc[j], o);
    float* red = (float*)sm;
    const int w = t >> 6, lane = t & 63;
    if (lane < 4) {
#pragma unroll
      for (int j = 0; j < 8; ++j) red[w * 32 + lane * 8 + j] = acc[j];
    }
    __syncthreads();
    if (t < 32) {
      float r = rb(red[t] + red[32 + t] + red[64 + t] + red[96 + t]);
      int n = 32 * cb + t;
      ((float*)(ws + WS_MOD))[n] = r;
      int seg = n >> 9, d = n & 511;
      if (seg == 1) ((float*)(ws + WS_OPC))[d] = rb(1.f + r);
      if (seg == 4) ((float*)(ws + WS_OPC))[512 + d] = rb(1.f + r);
      if (seg == 3) ((u16*)(ws + WS_SBB))[d] = (u16)(__float_as_uint(r) >> 16);
    }
  }
  STAMP(0, 2);
  STAMP_FLUSH(b < K0_NA ? 0 : b < K0_NA + K0_NB ? 4 : 5);
}

constexpr int K1_CS = 4160;
constexpr int K1_FC = 0;
constexpr int K1_U = 66560;
constexpr int K1_PART = K1_U + 2 * 4608;
constexpr int K1_WSTW = 16 * 288 + 16 * 144;
constexpr int K1_WST = K1_PART + 36864;
constexpr int K1_RED = K1_WST + 4 * K1_WSTW;
constexpr int K1_SC = K1_RED + 8192;
constexpr int K1_LDS = K1_SC + 512;

DI constexpr int conv_n(int i) {
  return i < 8 ? 0 : i < 15 ? 1 : i < 21 ? 2 : i < 26 ? 3 : i < 30 ? 4 : i == 30 ? 5 : i == 31 ? 7 : i < 34 ? 5 : 6;
}
DI constexpr int conv_d(int i) {
  return i < 8 ? i : i < 15 ? i - 8 : i < 21 ? i - 15 : i < 26 ? i - 21 : i < 30 ? i - 26 : i < 32 ? 0 : i < 34 ? i - 31 : i - 34;
}

template <bool CONV>
DI void k1_body(const u16* __restrict__ kf, const u16* __restrict__ ssmD, const u16* __restrict__ wg,
                const u16* __restrict__ wu, const u16* __restrict__ wd, char* __restrict__ ws, char* sm, const int w) {
  const int b = blockIdx.x, t = threadIdx.x;
  STAMP_DECL
  STAMP(1, 0);
  const int xcd = b & 7, bi = b >> 3;
  const int p = xcd * 32 + bi;
  const int lane = t & 63;
  const int ci = t & 255;
  const bool has_gu = bi < 28;
  const int it = xcd * 28 + (has_gu ? bi : 0);
  const int slab = it >> 3, kc = it & 7;
  const int g = lane >> 4, h = lane >> 5, qq = (lane & 15) >> 2, pp = lane & 3;

  if (CONV) {
    u32x4 ka[2], kb[2], uu[2];
#pragma unroll
    for (int ch = 0; ch < 2; ++ch) {
      const u16* kfr = kf + (size_t)(2 * p + ch) * S;
      ka[ch] = *(const u32x4*)(kfr + (ci > 0 ? ci - 1 : 0) * 8);
      kb[ch] = *(const u32x4*)(kfr + ci * 8);
    }
#pragma unroll
    for (int ch = 0; ch < 2; ++ch)
      uu[ch] = *(const u32x4*)((const u16*)(ws + WS_URT) + (size_t)(2 * p + ch) * S + ci * 8);
    const u32 dpair = ((const u32*)ssmD)[p];
    SCHED_FENCE();
#pragma unroll
    for (int ch = 0; ch < 2; ++ch) {
      u32 wv[8] = {ka[ch][0], ka[ch][1], ka[ch][2], ka[ch][3], kb[ch][0], kb[ch][1], kb[ch][2], kb[ch][3]};
#pragma unroll
      for (int i = 0; i < 4; ++i) wv[i] = ci > 0 ? wv[i] : 0u;
      u32 od[7];
#pragma unroll
      for (int i = 0; i < 7; ++i) od[i] = __builtin_amdgcn_alignbit(wv[i + 1], wv[i], 16);
      char* base = sm + K1_FC + ch * (8 * K1_CS) + (ci + 1) * 16;
#pragma unroll
      for (int r = 0; r < 8; ++r) {
        const int sg = r + 1;
        u32x4 val;
        if (sg & 1) {
          const int k0 = (sg - 1) / 2;
          val = (u32x4){od[k0], od[k0 + 1], od[k0 + 2], od[k0 + 3]};
        } else {
          const int k0 = sg / 2;
          val = (u32x4){wv[k0], wv[k0 + 1], wv[k0 + 2], wv[k0 + 3]};
        }
        *(u32x4*)(base + r * K1_CS) = val;
      }
    }
#pragma unroll
    for (int ch = 0; ch < 2; ++ch) *(u32x4*)(sm + K1_U + ch * 4608 + ci * 16) = uu[ch];
    const int cch = w >> 1, c0 = 4 * (w & 1);
    const int l15 = lane & 15;
    const char* fb = sm + K1_FC + cch * (8 * K1_CS) + (l15 & 7) * K1_CS + 2 * ((l15 & 8) + 8 * g) + 64 * c0;
    const char* gb = sm + K1_U + cch * 4608 + 2 * (2032 - 256 * 7 - 16 * l15 + 8 * g) + 64 * c0;
    u32x4 df;
    {
      const u32 dv = c0 == 0 ? (cch ? (dpair >> 16) : (dpair & 0xffffu)) : 0u;
      const int kap = 15 - l15 - 8 * g;
#pragma unroll
      for (int j = 0; j < 4; ++j) df[j] = (kap == 2 * j) ? dv : (kap == 2 * j + 1) ? (dv << 16) : 0u;
    }
    const unsigned wo1 = ((unsigned)(16 * w + (lane >> 3)) * F + 8 * (lane & 7)) * 2;
    const unsigned wo2 = ((unsigned)(16 * w + (lane >> 3)) * D + 8 * (lane & 7)) * 2;
    const char* wgb = (const char*)(wg + (size_t)(64 * kc) * F + 64 * slab);
    const char* wub = (const char*)(wu + (size_t)(64 * kc) * F + 64 * slab);
    const char* wdb = (const char*)(wd + (size_t)(64 * slab) * D + 64 * kc);
    asm volatile("" : "+v"(df));
    STAMP(1, 1);
    RAWBAR();
    STAMP(1, 2);
    f32x4 acc[8];
    bf16x8 Fr[2][8], Gr[2][8];
#pragma unroll
    for (int n = 0; n < 8; ++n) Gr[0][n] = *(const bf16x8*)(gb + 512 * (7 - n));
#pragma unroll
    for (int n = 0; n < 8; ++n) Fr[0][n] = *(const bf16x8*)(fb + 512 * n);
    SCHED_FENCE();
    u32x4 wl[6];
    wl[0] = *(const u32x4*)(wgb + wo1);
    wl[1] = *(const u32x4*)(wgb + 8 * F * 2 + wo1);
    wl[2] = *(const u32x4*)(wub + wo1);
    wl[3] = *(const u32x4*)(wub + 8 * F * 2 + wo1);
    wl[4] = *(const u32x4*)(wdb + wo2);
    wl[5] = *(const u32x4*)(wdb + 8 * D * 2 + wo2);
    SCHED_FENCE();
#pragma unroll
    for (int m = 0; m < 8; ++m)
      acc[m] = __builtin_amdgcn_mfma_f32_16x16x32_bf16(__builtin_bit_cast(bf16x8, df), Gr[0][m],
                                                       (f32x4){0.f, 0.f, 0.f, 0.f}, 0, 0, 0);
    SCHED_FENCE();
#pragma unroll
    for (int c = 0; c < 4; ++c) {
      const int cur = c & 1, nxt = cur ^ 1;
#pragma unroll
      for (int i = 0; i < 36; ++i) {
        const int n = conv_n(i), dl = conv_d(i);
        acc[n + dl] = __builtin_amdgcn_mfma_f32_16x16x32_bf16(Fr[cur][n], Gr[cur][dl], acc[n + dl], 0, 0, 0);
        if (c < 3 && (i & 1) && i < 32) {
          const int j = i >> 1;
          if (j == 0) Fr[nxt][0] = *(const bf16x8*)(fb + 64 * (c + 1));
          else if (j <= 8) Gr[nxt][j - 1] = *(const bf16x8*)(gb + 512 * (8 - j) + 64 * (c + 1));
          else Fr[nxt][j - 8] = *(const bf16x8*)(fb + 512 * (j - 8) + 64 * (c + 1));
        }
        if (i & 1) SCHED_FENCE();
      }
    }
    float* part = (float*)(sm + K1_PART) + w * (8 * 288) + g * 72 + l15 * 4;
#pragma unroll
    for (int m = 0; m < 8; ++m) *(f32x4*)(part + m * 288) = acc[m];
    STAMP(1, 3);
    RAWBAR();
    STAMP(1, 4);
    SCHED_FENCE();
    char* wst = sm + K1_WST + w * K1_WSTW;
    {
      const int row = lane >> 3, c16 = lane & 7;
      char* d1 = wst + row * 288 + c16 * 16;
      *(u32x4*)(d1) = wl[0];
      *(u32x4*)(d1 + 8 * 288) = wl[1];
      *(u32x4*)(d1 + 128) = wl[2];
      *(u32x4*)(d1 + 8 * 288 + 128) = wl[3];
      char* d2 = wst + 16 * 288 + row * 144 + c16 * 16;
      *(u32x4*)(d2) = wl[4];
      *(u32x4*)(d2 + 8 * 144) = wl[5];
    }
    __builtin_amdgcn_fence(__ATOMIC_RELEASE, "wavefront");
    __builtin_amdgcn_wave_barrier();
    __builtin_amdgcn_fence(__ATOMIC_ACQUIRE, "wavefront");
    if (has_gu) {
      const f32x4 sc0 = *(const f32x4*)(sm + K1_SC + (16 * w + 8 * h) * 4);
      const f32x4 sc1 = *(const f32x4*)(sm + K1_SC + (16 * w + 8 * h + 4) * 4);
      const u32x4 sbv = *(const u32x4*)(sm + K1_SC + 256 + (16 * w + 8 * h) * 2);
      const int ks = 4 * kc + w;
#pragma unroll
      for (int j = 0; j < 4; ++j) {
        const int mtg = 4 * slab + j;
        const char* a0 = wst + (8 * h + qq) * 288 + (g & 1) * 128 + j * 32 + pp * 8;
        s16x4 lo = __builtin_amdgcn_ds_read_tr16_b64_v4i16((lds_s4p)(a0));
        s16x4 hi = __builtin_amdgcn_ds_read_tr16_b64_v4i16((lds_s4p)(a0 + 4 * 288));
        u32x2 l2 = __builtin_bit_cast(u32x2, lo), h2 = __builtin_bit_cast(u32x2, hi);
        u32 wv[4] = {l2[0], l2[1], h2[0], h2[1]};
        u32x4 o;
        o[0] = pk2(lo2f(wv[0]) * sc0[0], hi2f(wv[0]) * sc0[1]);
        o[1] = pk2(lo2f(wv[1]) * sc0[2], hi2f(wv[1]) * sc0[3]);
        o[2] = pk2(lo2f(wv[2]) * sc1[0], hi2f(wv[2]) * sc1[1]);
        o[3] = pk2(lo2f(wv[3]) * sc1[2], hi2f(wv[3]) * sc1[3]);
        float cs = 0.f, bi2 = 0.f;
#pragma unroll
        for (int e = 0; e < 4; ++e) {
          cs = dot2(o[e], 0x3f803f80u, cs);
          bi2 = dot2(wv[e], sbv[e], bi2);
        }
        st16(ws + WS_W1F + (size_t)(mtg / 7) * W1F_CB + (size_t)ks * 7168 + (mtg % 7) * 1024 + lane * 16, o);
        f32x2 r = {cs, bi2};
        *(f32x2*)(sm + K1_RED + ((w * 4 + j) * 64 + lane) * 8) = r;
      }
#pragma unroll
      for (int j = 0; j < 2; ++j) {
        const char* a0 = wst + 16 * 288 + (4 * h + qq) * 144 + j * 64 + (g & 1) * 32 + pp * 8;
        s16x4 lo = __builtin_amdgcn_ds_read_tr16_b64_v4i16((lds_s4p)(a0));
        s16x4 hi = __builtin_amdgcn_ds_read_tr16_b64_v4i16((lds_s4p)(a0 + 8 * 144));
        u32x2 l2 = __builtin_bit_cast(u32x2, lo), h2 = __builtin_bit_cast(u32x2, hi);
        u32x4 o = {l2[0], l2[1], h2[0], h2[1]};
        st16(ws + WS_WDF + ((size_t)(2 * kc + j) * 112 + 4 * slab + w) * 1024 + lane * 16, o);
      }
      __builtin_amdgcn_fence(__ATOMIC_RELEASE, "workgroup");
      u32 old = 0;
      if (lane == 0) old = __hip_atomic_fetch_add((u32*)(sm + K1_SC + 384), 1u, __ATOMIC_RELAXED, __HIP_MEMORY_SCOPE_WORKGROUP);
      old = __builtin_amdgcn_readfirstlane(old);
      if (old == 3) {
        __builtin_amdgcn_fence(__ATOMIC_ACQUIRE, "workgroup");
        const f32x2* red = (const f32x2*)(sm + K1_RED);
#pragma unroll
        for (int jj = 0; jj < 2; ++jj) {
          const int j = 2 * jj + h, r31 = lane & 31;
          f32x2 rr = {0.f, 0.f};
#pragma unroll
          for (int i = 0; i < 4; ++i) rr += red[(i * 4 + j) * 64 + r31] + red[(i * 4 + j) * 64 + 32 + r31];
          const int mtg = 4 * slab + j;
          *(f32x2*)(ws + WS_CSP + (((size_t)(mtg / 7) * 8 + kc) * 224 + (mtg % 7) * 32 + r31) * 8) = rr;
        }
      }
    }
  } else {
    const float* mod = (const float*)(ws + WS_MOD);
    const float* opc = (const float*)(ws + WS_OPC);
    u32 qp[8];
    f32x2 st[8];
#pragma unroll
    for (int i = 0; i < 8; ++i) {
      qp[i] = ((const u32*)(ws + WS_QT2))[(size_t)p * S + ci + 256 * i];
      st[i] = ((const f32x2*)(ws + WS_STATS))[ci + 256 * i];
    }
    const char* sp = ci < 16 ? ws + WS_OPC + 2048 + kc * 256 + ci * 16 : ws + WS_SBB + kc * 128 + (ci & 7) * 16;
    const u32x4 scv = *(const u32x4*)sp;
    const float sa0 = mod[2 * p], sa1 = mod[2 * p + 1];
    const float oc0 = opc[2 * p], oc1 = opc[2 * p + 1];
    const float ga0 = mod[1024 + 2 * p], ga1 = mod[1024 + 2 * p + 1];
    SCHED_FENCE();
    {
      const u32x4 z = {0, 0, 0, 0};
      if (ci < 16) *(u32x4*)(sm + K1_FC + ci * K1_CS) = z;
      if (ci >= 64 && ci < 128) *(u32x4*)(sm + K1_U + ((ci >> 5) & 1) * 4608 + 4096 + (ci & 31) * 16) = z;
      if (ci == 128) *(u32x4*)(sm + K1_SC + 384) = z;
      if (ci >= 192 && ci < 196) *(u32*)(ws + WS_FLG + (size_t)(b * 4 + ci - 192) * 64) = 0u;
      if (ci >= 200 && ci < 204) *(u32x4*)(ws + WS_ST2 + (size_t)(b * 4 + ci - 200) * 16) = z;
    }
    float h0[8], h1[8];
#pragma unroll
    for (int i = 0; i < 8; ++i) {
      u32 x = pk2(lo2f(qp[i]) - st[i][0], hi2f(qp[i]) - st[i][0]);
      x = pk2(lo2f(x) * st[i][1], hi2f(x) * st[i][1]);
      x = pk2(lo2f(x) * oc0, hi2f(x) * oc1);
      x = pk2(lo2f(x) + sa0, hi2f(x) + sa1);
      h0[i] = lo2f(x);
      h1[i] = hi2f(x);
      asm volatile("" : "+v"(h0[i]), "+v"(h1[i]));
    }
    if (ci < 24) *(u32x4*)(sm + K1_SC + ci * 16) = scv;
    STAMP(1, 1);
    RAWBAR();
    STAMP(1, 2);
    RAWBAR();
    STAMP(1, 4);
    const float* part = (const float*)(sm + K1_PART) + ((ci >> 2) & 3) * 72 + ((ci >> 4) & 15) * 4 + (ci & 3);
    const int d0 = 2 * p;
    char* dst = ws + WS_X1F + ((size_t)(d0 >> 4) * 64 + 32 * ((d0 >> 3) & 1)) * 16 + 2 * (d0 & 7) +
                (size_t)(ci >> 5) * 32768 + (ci & 31) * 16;
#pragma unroll
    for (int i = 0; i < 8; ++i) {
      float y0 = part[i * 288] + part[2304 + i * 288];
      float y1 = part[2 * 2304 + i * 288] + part[3 * 2304 + i * 288];
      u32 z = pk2(y0, y1);
      u32 a = pk2(h0[i] * lo2f(z), h1[i] * hi2f(z));
      a = pk2(ga0 * lo2f(a), ga1 * hi2f(a));
      u32 x1 = pk2(lo2f(qp[i]) + lo2f(a), hi2f(qp[i]) + hi2f(a));
      *(u32*)(dst + (size_t)i * (8 * 32768)) = x1;
    }
  }
  STAMP(1, 5);
  STAMP_FLUSH(1);
}

__global__ __launch_bounds__(512, 2) void k1_conv(const u16* __restrict__ kf, const u16* __restrict__ ssmD,
                                                  const u16* __restrict__ wg, const u16* __restrict__ wu,
                                                  const u16* __restrict__ wd, char* __restrict__ ws) {
  extern __shared__ __attribute__((aligned(16))) char sm[];
  const int w = __builtin_amdgcn_readfirstlane(threadIdx.x >> 6);
  if (w < 4) k1_body<true>(kf, ssmD, wg, wu, wd, ws, sm, w);
  else k1_body<false>(kf, ssmD, wg, wu, wd, ws, sm, w);
}

constexpr int K2_G = 28672;
constexpr int K2_NS = 4;
constexpr int K2_COR = K2_NS * K2_G;
constexpr int K2_FREE = K2_COR + 8192;
constexpr int K2_LDS = 163840;

#define GLOAD(dst, voff, sbase, imm) \
  asm volatile("global_load_dwordx4 %0, %1, %2 offset:" #imm : "=v"(dst) : "v"(voff), "s"(sbase) : "memory")
#define GDMA(voff, sbase, ldsa, imm)                                                               \
  asm volatile("s_mov_b32 m0, %2\n\ts_nop 0\n\tglobal_load_lds_dwordx4 %0, %1 offset:" #imm       \
               :: "v"(voff), "s"(sbase), "s"(ldsa) : "memory")

DI constexpr int wd_lds_off(int q, int w) {
  return q < 10 ? K2_FREE + w * 10240 + q * 1024
       : q < 17 ? 0 * K2_G + w * 7168 + (q - 10) * 1024
       : q < 24 ? 1 * K2_G + w * 7168 + (q - 17) * 1024
                : 2 * K2_G + w * 7168 + (q - 24) * 1024;
}

__global__ __launch_bounds__(256, 1) __attribute__((amdgpu_waves_per_eu(1, 1))) void k23_mlp(char* __restrict__ ws,
                                                                                             u16* __restrict__ out) {
  extern __shared__ __attribute__((aligned(16))) char sm[];
  const int b = blockIdx.x, t = threadIdx.x;
  STAMP_DECL
  STAMP(2, 0);
  const int w = __builtin_amdgcn_readfirstlane(t >> 6), lane = t & 63;
  const int xcd = b & 7, idx = b >> 3;
  const int rbk = (xcd & 1) * 8 + (idx & 7);
  const int cbk = (xcd >> 1) * 4 + (idx >> 3);
  const int st = rbk * 4 + w;
  const int sbk = xcd * 4 + (idx & 3);
  const int nbk = idx >> 2;
  const char* wsrc = ws + WS_W1F + (size_t)cbk * W1F_CB + w * 7168;
  const char* asrc = ws + WS_X1F + (size_t)st * 32768;
  const char* dsrc = ws + WS_WDF + ((size_t)(2 * nbk) * 112 + 28 * w) * 1024;
  const unsigned lbase = (unsigned)(uintptr_t)(__attribute__((address_space(3))) char*)sm;
  const unsigned ldst = lbase + w * 7168;
  const unsigned voff = lane * 16;

  u32x4 act[32];
#define VM(g, j)                                                                                   \
  {                                                                                                \
    if ((j) == 0) GLOAD(act[4 * (g) + 0], voff, asrc + (g) * 4096, 0);                             \
    if ((j) == 1) GLOAD(act[4 * (g) + 1], voff, asrc + (g) * 4096, 1024);                          \
    if ((j) == 2) GLOAD(act[4 * (g) + 2], voff, asrc + (g) * 4096, 2048);                          \
    if ((j) == 3) GLOAD(act[4 * (g) + 3], voff, asrc + (g) * 4096, 3072);                          \
    if ((j) == 4) GDMA(voff, wsrc + (g) * K2_G, ldst + ((g) % K2_NS) * K2_G, 0);                   \
    if ((j) == 5) GDMA(voff, wsrc + (g) * K2_G, ldst + ((g) % K2_NS) * K2_G, 1024);                \
    if ((j) == 6) GDMA(voff, wsrc + (g) * K2_G, ldst + ((g) % K2_NS) * K2_G, 2048);                \
    if ((j) == 7) GDMA(voff, wsrc + (g) * K2_G, ldst + ((g) % K2_NS) * K2_G, 3072);                \
    if ((j) == 8) GDMA(voff, wsrc + (g) * K2_G + 4096, ldst + ((g) % K2_NS) * K2_G + 4096, 0);     \
    if ((j) == 9) GDMA(voff, wsrc + (g) * K2_G + 4096, ldst + ((g) % K2_NS) * K2_G + 4096, 1024);  \
    if ((j) == 10) GDMA(voff, wsrc + (g) * K2_G + 4096, ldst + ((g) % K2_NS) * K2_G + 4096, 2048); \
  }
#define VMALL(g) { VM(g,4) VM(g,0) VM(g,5) VM(g,6) VM(g,7) VM(g,8) VM(g,9) VM(g,10) VM(g,1) VM(g,2) VM(g,3) }
#define WDM(q) { if ((q) >= 0 && (q) < 30) GDMA(voff, dsrc + ((q) & 1) * (112 * 1024) + ((q) >> 1) * 1024, lbase + wd_lds_off((q) < 0 ? 0 : (q), w), 0); }
  u32x4 sx[2];
  u32x2 cp[8];
  {
    const char* sbase = ws + WS_X1F + (size_t)(rbk * 4 + (cbk >> 2)) * 32768 + (size_t)w * 8192 + (cbk & 3) * 128;
    const unsigned so = (lane >> 3) * 512 + (lane & 7) * 16;
    GLOAD(sx[0], so, sbase, 0);
    GLOAD(sx[1], so, sbase + 4096, 0);
    const char* cbase = ws + WS_CSP + (size_t)cbk * (8 * 224 * 8);
    const unsigned co = (t < 224 ? t : 0) * 8;
#pragma unroll
    for (int i = 0; i < 8; ++i)
      asm volatile("global_load_dwordx2 %0, %1, %2" : "=v"(cp[i]) : "v"(co), "s"(cbase + i * 1792) : "memory");
  }
  VMALL(0) VMALL(1) VMALL(2)

  f32x16 acc[7];
#pragma unroll
  for (int m = 0; m < 7; ++m)
#pragma unroll
    for (int r = 0; r < 16; ++r) acc[m][r] = 0.f;
  const char* lsrc = sm + lane * 16;
  bf16x8 wf[2][7];

#define WAITG(g, n)                                                                                           \
  SCHED_FENCE();                                                                                              \
  asm volatile("s_waitcnt vmcnt(" #n ") lgkmcnt(0)"                                                           \
               : "+v"(act[4 * (g)]), "+v"(act[4 * (g) + 1]), "+v"(act[4 * (g) + 2]), "+v"(act[4 * (g) + 3]) \
               :                                                                                              \
               : "memory");                                                                                   \
  __builtin_amdgcn_s_barrier();                                                                               \
  asm volatile("" ::: "memory");                                                                              \
  SCHED_FENCE();
#define KOFF(i) ((i) == 32 ? K2_COR : (((i) >> 2) % K2_NS) * K2_G + ((i) & 3) * 7168)
#define RD(i, m) wf[(i) & 1][m] = *(const bf16x8*)(lsrc + KOFF(i) + (m) * 1024);
#define MM(i, m) acc[m] = __builtin_amdgcn_mfma_f32_32x32x16_bf16(wf[(i) & 1][m], __builtin_bit_cast(bf16x8, act[i]), acc[m], 0, 0, 0);
#define VX(vg, j0, q0, k) { if ((vg) >= 0) VM((vg) < 0 ? 0 : (vg), (j0) + (k)) else WDM((q0) < 0 ? -1 : (q0) + (k)) }
  u32x4 wr2[26];
#define WDR(r) { if ((r) >= 0 && (r) < 26) GLOAD(wr2[(r) < 0 ? 0 : (r) % 26], voff, dsrc + ((r) & 1) * (112 * 1024) + (15 + ((r) >> 1)) * 1024, 0); }
#define STEPR(i, vg, j0, q0, r0)                                                 \
  {                                                                              \
    MM(i, 0) RD((i) + 1, 0) RD((i) + 1, 1) SCHED_FENCE();                        \
    MM(i, 1) RD((i) + 1, 2) RD((i) + 1, 3) WDR(r0) SCHED_FENCE();                \
    MM(i, 2) RD((i) + 1, 4) RD((i) + 1, 5) WDR((r0) < 0 ? -1 : (r0) + 1) SCHED_FENCE(); \
    MM(i, 3) RD((i) + 1, 6) WDR((r0) < 0 ? -1 : (r0) + 2) SCHED_FENCE();         \
    MM(i, 4) VX(vg, j0, q0, 0) SCHED_FENCE();                                    \
    MM(i, 5) VX(vg, j0, q0, 1) SCHED_FENCE();                                    \
    MM(i, 6) VX(vg, j0, q0, 2) SCHED_FENCE();                                    \
  }
#define STEP(i, vg, j0, q0) STEPR(i, vg, j0, q0, -1)
  SCHED_FENCE();
  asm volatile("s_waitcnt vmcnt(33)"
               : "+v"(sx[0]), "+v"(sx[1]), "+v"(cp[0]), "+v"(cp[1]), "+v"(cp[2]), "+v"(cp[3]), "+v"(cp[4]), "+v"(cp[5]),
                 "+v"(cp[6]), "+v"(cp[7])
               :
               : "memory");
  {
    float sum = 0.f, sq = 0.f;
#pragma unroll
    for (int i = 0; i < 2; ++i)
#pragma unroll
      for (int j = 0; j < 4; ++j) {
        sum = dot2(sx[i][j], 0x3f803f80u, sum);
        sq = dot2(sx[i][j], sx[i][j], sq);
      }
    sum = red_8_16_32(sum);
    sq = red_8_16_32(sq);
    if (lane < 8) *(f32x2*)(sm + K2_COR + 7168 + (w * 8 + lane) * 8) = (f32x2){sum, sq};
    float cs = 0.f, bs = 0.f;
#pragma unroll
    for (int i = 0; i < 8; ++i) {
      cs += __uint_as_float(cp[i][0]);
      bs += __uint_as_float(cp[i][1]);
    }
    const float c0 = rb(cs), c1 = rb(cs - c0), c2 = rb(cs - c0 - c1);
    const float b0 = rb(bs), b1 = rb(bs - b0), b2 = rb(bs - b0 - b1);
    if (t < 224) {
      char* cd = sm + K2_COR + (t >> 5) * 1024 + (t & 31) * 16;
      *(u32x4*)cd = (u32x4){pk2(c0, c1), pk2(c2, b0), pk2(b1, b2), 0u};
      *(u32x4*)(cd + 512) = (u32x4){0u, 0u, 0u, 0u};
    }
  }
  WAITG(0, 22)
  STAMP(2, 1);
  RD(0, 0) RD(0, 1) RD(0, 2) RD(0, 3) RD(0, 4) RD(0, 5) RD(0, 6)
  if (w == 0) {
    if (lane < 8) {
      const f32x2* ps = (const f32x2*)(sm + K2_COR + 7168);
      const f32x2 r = ps[lane] + ps[8 + lane] + ps[16 + lane] + ps[24 + lane];
      const float mean = r[0] * (1.f / D);
      const float var = fmaxf(r[1] * (1.f / D) - mean * mean, 0.f);
      const float sdv = __builtin_amdgcn_sqrtf(rb(rb(var) + 1e-5f));
      const unsigned long long gr = (1ull << 32) | pk2(mean, sdv);
      __hip_atomic_store((unsigned long long*)(ws + WS_ST2) + rbk * 128 + cbk * 8 + lane, gr, __ATOMIC_RELAXED,
                         __HIP_MEMORY_SCOPE_AGENT);
    }
  }
  VM(3, 0) VM(3, 1)
  SCHED_FENCE();
  STEP(0, 3, 2, -1) STEP(1, 3, 5, -1) STEP(2, 3, 8, -1)
  WAITG(1, 22)
  STEP(3, 4, 0, -1) STEP(4, 4, 3, -1) STEP(5, 4, 6, -1) STEP(6, 4, 9, -1)
  WAITG(2, 22)
  STEP(7, 5, 0, -1) STEP(8, 5, 3, -1) STEP(9, 5, 6, -1) STEP(10, 5, 9, -1)
  WAITG(3, 22)
  STEP(11, 6, 0, -1) STEP(12, 6, 3, -1) STEP(13, 6, 6, -1) STEP(14, 6, 9, -1)
  WAITG(4, 22)
  STEP(15, 7, 0, -1) STEP(16, 7, 3, -1) STEP(17, 7, 6, -1) STEP(18, 7, 9, -1)
  u32x2 stg;
  const char* stp = ws + WS_ST2 + (size_t)st * 256;
  const unsigned sto = (lane & 31) * 8;
  asm volatile("global_load_dwordx2 %0, %1, %2 sc1" : "=v"(stg) : "v"(sto), "s"(stp) : "memory");
  WAITG(5, 23)
  STEP(19, -1, 0, 0) STEP(20, -1, 0, 3) STEP(21, -1, 0, 6) STEP(22, -1, 0, 9)
  WAITG(6, 24)
  STEP(23, -1, 0, 12) STEP(24, -1, 0, 15) STEP(25, -1, 0, 18) STEP(26, -1, 0, 21)
  WAITG(7, 24)
  asm volatile("" : "+v"(stg));
  for (int spins = 0; !__all(stg[1] == 1u) && spins < 20000; ++spins) {
    __builtin_amdgcn_s_sleep(1);
    asm volatile("global_load_dwordx2 %0, %1, %2 sc1\n\ts_waitcnt vmcnt(0)" : "=&v"(stg) : "v"(sto), "s"(stp) : "memory");
  }
  SCHED_FENCE();
  STEP(27, -1, 0, 24)
  bf16x8 tl[2][4];
#define RDT(m, k) tl[(m) & 1][k] = *(const bf16x8*)(lsrc + KOFF(29 + (k)) + (m) * 1024);
  const float mb = lo2f(stg[0]), sd = hi2f(stg[0]);
  float rs2;
  {
    MM(28, 0) RDT(0, 0) RDT(0, 1) SCHED_FENCE();
    MM(28, 1) RDT(0, 2) RDT(0, 3) SCHED_FENCE();
    MM(28, 2) WDM(27) SCHED_FENCE();
    MM(28, 3) WDM(28) SCHED_FENCE();
    MM(28, 4) WDM(29) SCHED_FENCE();
    MM(28, 5)
    const float rs = __builtin_amdgcn_rcpf(sd);
    rs2 = rs * rs;
    SCHED_FENCE();
    MM(28, 6) SCHED_FENCE();
  }
  STAMP(2, 2);
  const u32x4 cb4 = {pk2(-mb, -mb), pk2(-mb, sd), pk2(sd, sd), 0u};
  const auto wsr = __builtin_amdgcn_make_buffer_rsrc(ws, 0, 0x7fffffff, 0x00020000);
  const unsigned hoff = (unsigned)WS_HHF + ((unsigned)st * 112 + cbk * 7) * 1024;
  u32x4 eo;
#define EPJ(m, j)                                                                     \
  {                                                                                   \
    float u0 = acc[m][8 + 2 * (j)], u1 = acc[m][8 + 2 * (j) + 1];                     \
    asm volatile("" : "+v"(u0), "+v"(u1));                                            \
    float e0 = relu(acc[m][2 * (j)]) * u0 * rs2;                                      \
    float e1 = relu(acc[m][2 * (j) + 1]) * u1 * rs2;                                  \
    eo[j] = pk2(e0, e1);                                                              \
  }
#define EST(m) __builtin_amdgcn_raw_buffer_store_b128(eo, wsr, voff + (m) * 1024, hoff, 16);
#define MT(m, k, bop) acc[m] = __builtin_amdgcn_mfma_f32_32x32x16_bf16(tl[(m) & 1][k], __builtin_bit_cast(bf16x8, bop), acc[m], 0, 0, 0);
#define TAIL(m)                                                                                      \
  {                                                                                                  \
    MT(m, 0, act[29]) SCHED_FENCE();                                                                 \
    if ((m) < 6) { RDT((m) + 1, 0) RDT((m) + 1, 1) }                                                 \
    SCHED_FENCE();                                                                                   \
    MT(m, 1, act[30]) SCHED_FENCE();                                                                 \
    if ((m) < 6) { RDT((m) + 1, 2) RDT((m) + 1, 3) }                                                 \
    if ((m) > 0) EPJ((m) > 0 ? (m) - 1 : 0, 0)                                                       \
    SCHED_FENCE();                                                                                   \
    MT(m, 2, act[31]) SCHED_FENCE();                                                                 \
    if ((m) > 0) { EPJ((m) > 0 ? (m) - 1 : 0, 1) EPJ((m) > 0 ? (m) - 1 : 0, 2) }                     \
    SCHED_FENCE();                                                                                   \
    MT(m, 3, cb4) SCHED_FENCE();                                                                     \
    if ((m) > 0) { EPJ((m) > 0 ? (m) - 1 : 0, 3) EST((m) > 0 ? (m) - 1 : 0) }                        \
    SCHED_FENCE();                                                                                   \
  }
  TAIL(0) TAIL(1) TAIL(2) TAIL(3) TAIL(4) TAIL(5) TAIL(6)
  EPJ(6, 0) EPJ(6, 1) EPJ(6, 2) EPJ(6, 3) EST(6)
  asm volatile("s_waitcnt vmcnt(0)" ::: "memory");
  u32* flg = (u32*)(ws + WS_FLG);
  if (lane == 0) __hip_atomic_store(flg + (st * 16 + cbk) * 16, 1u, __ATOMIC_RELAXED, __HIP_MEMORY_SCOPE_AGENT);
  STAMP(2, 3);
  SCHED_FENCE();

#pragma unroll
  for (int r = 0; r < 26; ++r) wr2[r] = *(const u32x4*)(dsrc + (r & 1) * (112 * 1024) + (15 + (r >> 1)) * 1024 + voff);
  const int fn = w >> 1, fs = w & 1;
  const int h = lane >> 5;
  const int ncol = 64 * nbk + 32 * fn;
  u32x4 xr[2];
  f32x4 gv[4];
  {
    const char* xsrc = ws + WS_X1F + ((size_t)(2 * sbk + fs) * 32 + (ncol >> 4)) * 1024 + lane * 16;
    xr[0] = *(const u32x4*)(xsrc);
    xr[1] = *(const u32x4*)(xsrc + 1024);
    const float* gbp = (const float*)(ws + WS_MOD) + 5 * D + ncol + 8 * h;
    gv[0] = *(const f32x4*)(gbp);
    gv[1] = *(const f32x4*)(gbp + 4);
    gv[2] = *(const f32x4*)(gbp + 16);
    gv[3] = *(const f32x4*)(gbp + 20);
  }
  SCHED_FENCE();
  {
    const u32* fp = flg + ((2 * sbk + (lane & 1)) * 16 + 4 * w + ((lane >> 1) & 3)) * 16;
    for (int spins = 0; spins < 20000; ++spins) {
      u32 f = __hip_atomic_load(fp, __ATOMIC_RELAXED, __HIP_MEMORY_SCOPE_AGENT);
      if (__all(f == 1u)) break;
      __builtin_amdgcn_s_sleep(1);
    }
    __builtin_amdgcn_fence(__ATOMIC_ACQUIRE, "wavefront");
  }
  STAMP(2, 4);
  SCHED_FENCE();
  const unsigned hs = (unsigned)WS_HHF + ((unsigned)(2 * sbk) * 112 + 28 * w) * 1024;
  constexpr int WIN = 16;
  u32x4 hb[WIN][2];
#define HLD(i)                                                                                          \
  {                                                                                                     \
    hb[(i) % WIN][0] = __builtin_amdgcn_raw_buffer_load_b128(wsr, voff, hs + (i) * 1024, 16);           \
    hb[(i) % WIN][1] = __builtin_amdgcn_raw_buffer_load_b128(wsr, voff, hs + (112 + (i)) * 1024, 16);   \
  }
#pragma unroll
  for (int i = 0; i < WIN; ++i) HLD(i)
  SCHED_FENCE();
  f32x16 ac2[2][2];
#pragma unroll
  for (int a = 0; a < 2; ++a)
#pragma unroll
    for (int c = 0; c < 2; ++c)
#pragma unroll
      for (int r = 0; r < 16; ++r) ac2[a][c][r] = 0.f;
  bf16x8 wl2[2][2];
  wl2[0][0] = *(const bf16x8*)(lsrc + wd_lds_off(0, w));
  wl2[0][1] = *(const bf16x8*)(lsrc + wd_lds_off(1, w));
#pragma unroll
  for (int i = 0; i < 28; ++i) {
    if (i + 1 < 15) {
      wl2[(i + 1) & 1][0] = *(const bf16x8*)(lsrc + wd_lds_off(2 * (i + 1), w));
      wl2[(i + 1) & 1][1] = *(const bf16x8*)(lsrc + wd_lds_off(2 * (i + 1) + 1, w));
    }
#pragma unroll
    for (int a = 0; a < 2; ++a)
#pragma unroll
      for (int c = 0; c < 2; ++c)
        ac2[a][c] = __builtin_amdgcn_mfma_f32_32x32x16_bf16(i < 15 ? wl2[i & 1][a] : __builtin_bit_cast(bf16x8, wr2[i < 15 ? 0 : 2 * (i - 15) + a]),
                                                            __builtin_bit_cast(bf16x8, hb[i % WIN][c]), ac2[a][c], 0, 0, 0);
    if (i == 0) STAMP(2, 5);
    if (i + WIN < 28) HLD(i + WIN)
    SCHED_FENCE();
  }
  STAMP(2, 6);
#define PARK(w_, j) ((j) == 1 ? K2_FREE + (w_) * 10240 : (j) == 2 ? K2_FREE + (w_) * 10240 + 4096 : (w_) * 7168)
#pragma unroll
  for (int a = 0; a < 2; ++a)
#pragma unroll
    for (int c = 0; c < 2; ++c) {
      const int tile = a * 2 + c;
      if (tile != w) {
        const int j = (tile - w) & 3;
        char* dst = sm + PARK(w, j) + lane * 16;
#pragma unroll
        for (int r = 0; r < 4; ++r)
          *(f32x4*)(dst + r * 1024) =
              (f32x4){ac2[a][c][4 * r], ac2[a][c][4 * r + 1], ac2[a][c][4 * r + 2], ac2[a][c][4 * r + 3]};
      }
    }
  __syncthreads();
  f32x16 fin;
  if (w == 0) fin = ac2[0][0];
  else if (w == 1) fin = ac2[0][1];
  else if (w == 2) fin = ac2[1][0];
  else fin = ac2[1][1];
#pragma unroll
  for (int o = 1; o < 4; ++o) {
    const int ow = (w + o) & 3;
    const char* src = sm + PARK(ow, 4 - o) + lane * 16;
#pragma unroll
    for (int r = 0; r < 4; ++r) {
      f32x4 v = *(const f32x4*)(src + r * 1024);
#pragma unroll
      for (int e = 0; e < 4; ++e) fin[4 * r + e] += v[e];
    }
  }
  u32 pkd[8];
#pragma unroll
  for (int j = 0; j < 8; ++j) pkd[j] = pk2(fin[2 * j], fin[2 * j + 1]);
  const int srow = 64 * sbk + 32 * fs + (lane & 31);
  u16* orow = out + (size_t)srow * D + ncol + 8 * h;
#pragma unroll
  for (int kk = 0; kk < 2; ++kk) {
    auto r0 = __builtin_amdgcn_permlane32_swap(pkd[4 * kk], pkd[4 * kk + 2], false, false);
    auto r1 = __builtin_amdgcn_permlane32_swap(pkd[4 * kk + 1], pkd[4 * kk + 3], false, false);
    u32 m4[4] = {r0[0], r1[0], r0[1], r1[1]};
    u32x4 o;
#pragma unroll
    for (int j = 0; j < 4; ++j) {
      float g0 = gv[2 * kk + (j >> 1)][(j & 1) * 2], g1 = gv[2 * kk + (j >> 1)][(j & 1) * 2 + 1];
      u32 a = pk2(g0 * lo2f(m4[j]), g1 * hi2f(m4[j]));
      o[j] = pk2(lo2f(xr[kk][j]) + lo2f(a), hi2f(xr[kk][j]) + hi2f(a));
    }
    st16(orow + 16 * kk, o);
  }
  STAMP(2, 7);
  STAMP_FLUSH(2);
}

__global__ void k_end(char* ws) {
  if (ws == nullptr) __builtin_trap();
}

extern "C" void kernel_launch(void* const* d_in, const int* in_sizes, int n_in, void* d_out, int out_size,
                              void* d_ws, size_t ws_size, hipStream_t stream) {
  const u16* q = (const u16*)d_in[0];
  const u16* k = (const u16*)d_in[1];
  const u16* v = (const u16*)d_in[2];
  const u16* shk = (const u16*)d_in[3];
  const u16* kf = (const u16*)d_in[4];
  const u16* sD = (const u16*)d_in[5];
  const u16* wg = (const u16*)d_in[7];
  const u16* wu = (const u16*)d_in[8];
  const u16* wd = (const u16*)d_in[9];
  const u16* c = (const u16*)d_in[10];
  const u16* wm = (const u16*)d_in[11];
  char* ws = (char*)d_ws;
  (void)hipFuncSetAttribute((const void*)k1_conv, hipFuncAttributeMaxDynamicSharedMemorySize, K1_LDS);
  (void)hipFuncSetAttribute((const void*)k23_mlp, hipFuncAttributeMaxDynamicSharedMemorySize, K2_LDS);
  k0_prep<<<K0_NA + K0_NB + K0_NC, 256, 0, stream>>>(q, k, v, shk, c, wm, kf, wg, wu, wd, ws);
  k1_conv<<<256, 512, K1_LDS, stream>>>(kf, sD, wg, wu, wd, ws);
  k23_mlp<<<256, 256, K2_LDS, stream>>>(ws, (u16*)d_out);
}
```

```cpp
#include <hip/hip_runtime.h>
#include <stdint.h>

typedef unsigned short u16;
typedef unsigned int u32;
typedef unsigned long long u64;
typedef __attribute__((ext_vector_type(8))) short bf16x8;
typedef __attribute__((ext_vector_type(4))) short s16x4;
typedef __attribute__((ext_vector_type(4))) float f32x4;
typedef __attribute__((ext_vector_type(2))) float f32x2;
typedef __attribute__((ext_vector_type(16))) float f32x16;
typedef __attribute__((ext_vector_type(4))) unsigned u32x4;
typedef __attribute__((ext_vector_type(2))) unsigned u32x2;
typedef __attribute__((ext_vector_type(2))) __bf16 bf2;
typedef __attribute__((address_space(3))) s16x4* lds_s4p;

#define DI __device__ __forceinline__

constexpr int S = 2048, D = 512, F = 1792;

DI float lo2f(u32 x) { return __uint_as_float(x << 16); }
DI float hi2f(u32 x) { return __uint_as_float(x & 0xffff0000u); }
DI u32 pk2(float a, float b) {
  f32x2 v = {a, b};
  return __builtin_bit_cast(u32, __builtin_convertvector(v, bf2));
}
DI float rb(float a) { return lo2f(pk2(a, a)); }
DI float dot2(u32 a, u32 b, float c) {
  return __builtin_amdgcn_fdot2_f32_bf16(__builtin_bit_cast(bf2, a), __builtin_bit_cast(bf2, b), c, false);
}
template <int CTRL>
DI float dpp_add(float x) {
  return x + __uint_as_float(__builtin_amdgcn_update_dpp(0u, __float_as_uint(x), CTRL, 0xf, 0xf, false));
}
DI float red_16_32(float x) {
  auto a = __builtin_amdgcn_permlane16_swap(__float_as_uint(x), __float_as_uint(x), false, false);
  x = __uint_as_float(a[0]) + __uint_as_float(a[1]);
  auto b = __builtin_amdgcn_permlane32_swap(__float_as_uint(x), __float_as_uint(x), false, false);
  return __uint_as_float(b[0]) + __uint_as_float(b[1]);
}
DI float red_8_16_32(float x) { return red_16_32(dpp_add<0x128>(x)); }
DI float red_2_64(float x) {
  x = dpp_add<0x4E>(x);
  x = dpp_add<0x124>(x);
  return red_8_16_32(x);
}
DI float wave_sum(float x) { return red_2_64(dpp_add<0xB1>(x)); }
DI float relu(float x) {
  float r;
  asm("v_max_f32 %0, 0, %1" : "=v"(r) : "v"(x));
  return r;
}

#define STAMP_DECL
#define STAMP(i)
#define STAMP_FLUSH()
#define SCHED_FENCE() __builtin_amdgcn_sched_barrier(0)
#define RAWBAR()                                          \
  do {                                                    \
    __builtin_amdgcn_sched_barrier(0);                    \
    asm volatile("s_waitcnt lgkmcnt(0)" ::: "memory");    \
    __builtin_amdgcn_s_barrier();                         \
    asm volatile("" ::: "memory");                        \
    __builtin_amdgcn_sched_barrier(0);                    \
  } while (0)
#define DRAINBAR()                                                 \
  do {                                                             \
    __builtin_amdgcn_sched_barrier(0);                             \
    asm volatile("s_waitcnt vmcnt(0) lgkmcnt(0)" ::: "memory");    \
    __builtin_amdgcn_s_barrier();                                  \
    asm volatile("" ::: "memory");                                 \
    __builtin_amdgcn_sched_barrier(0);                             \
  } while (0)

constexpr u32 WS_EPOCH = 0;
constexpr u32 WS_FLA = 4096;
constexpr u32 WS_FLQ = WS_FLA + 256 * 64;
constexpr u32 WS_FLW = WS_FLQ + 256 * 64;
constexpr u32 WS_FL1 = WS_FLW + 256 * 64;
constexpr u32 WS_FL2 = WS_FL1 + 256 * 64;
constexpr u32 WS_MODG = WS_FL2 + 1024 * 64;
constexpr u32 WS_ST1G = WS_MODG + 4 * 3072 * 8;
constexpr u32 WS_ST2G = WS_ST1G + 2048 * 8;
constexpr u32 WS_CSP = WS_ST2G + 2048 * 8;
constexpr u32 WS_URT = 1u << 20;
constexpr u32 WS_QT2 = WS_URT + 2u * S * D;
constexpr u32 WS_X1P = WS_QT2 + 2u * S * D;
constexpr u32 WS_W1F = WS_X1P + 2u * S * D;
constexpr u32 W1F_CB = 32 * 7 * 1024;
constexpr u32 WS_WDF = WS_W1F + 16 * W1F_CB;
constexpr u32 WS_HHF = WS_WDF + 2u * F * D;
constexpr u32 WS_END = WS_HHF + 2u * S * F;
static_assert(WS_CSP + 16 * 8 * 224 * 8 <= WS_URT, "ws layout");

constexpr int L_CS = 4160;
constexpr int L_FC = 0;
constexpr int L_TU = 66560;
constexpr int L_TQ = L_TU + 8192;
constexpr int L_WSTW = 16 * 288 + 16 * 144;
constexpr int L_WST = L_TQ + 8192;
constexpr int L_SCR = L_WST + 4 * L_WSTW;
constexpr int L_U = 114688;
constexpr int L_PART = L_U + 2 * 4608;
constexpr int L_RED = L_TU;
constexpr int G_W = 28672;
constexpr int L_A = 4 * G_W;
constexpr int G_A = 16384;
constexpr int L_COR = L_A + 2 * G_A;
constexpr int LDS_BYTES = 163840;
static_assert(L_SCR + 4096 <= L_U && L_PART + 36864 <= LDS_BYTES, "lds layout");

constexpr int SPIN_CAP = 30000;

#define BLD128(off, soff) __builtin_amdgcn_raw_buffer_load_b128(wsr, (off), (soff), 16)
#define BLD64(off, soff) __builtin_amdgcn_raw_buffer_load_b64(wsr, (off), (soff), 16)
#define BLD32(off, soff) __builtin_amdgcn_raw_buffer_load_b32(wsr, (off), (soff), 16)
#define BST128(v, off, soff) __builtin_amdgcn_raw_buffer_store_b128((v), wsr, (off), (soff), 16)
#define BST64(v, off, soff) __builtin_amdgcn_raw_buffer_store_b64((v), wsr, (off), (soff), 16)
#define BST32(v, off, soff) __builtin_amdgcn_raw_buffer_store_b32((v), wsr, (off), (soff), 16)
#define GDMA(voff, sbase, ldsa, imm)                                                                  \
  asm volatile("s_mov_b32 m0, %2\n\ts_nop 0\n\tglobal_load_lds_dwordx4 %0, %1 offset:" #imm " sc1"    \
               ::"v"(voff), "s"(sbase), "s"(ldsa) : "memory")

DI constexpr int conv_n(int i) {
  return i < 8 ? 0 : i < 15 ? 1 : i < 21 ? 2 : i < 26 ? 3 : i < 30 ? 4 : i == 30 ? 5 : i == 31 ? 7 : i < 34 ? 5 : 6;
}
DI constexpr int conv_d(int i) {
  return i < 8 ? i : i < 15 ? i - 8 : i < 21 ? i - 15 : i < 26 ? i - 21 : i < 30 ? i - 26 : i < 32 ? 0 : i < 34 ? i - 31 : i - 34;
}
DI constexpr int wd_lds_off(int q, int w) { return (q / 7) * G_W + w * 7168 + (q % 7) * 1024; }

__global__ __launch_bounds__(256, 1) __attribute__((amdgpu_waves_per_eu(1, 1))) void mega(
    const u16* __restrict__ q, const u16* __restrict__ k, const u16* __restrict__ v, const u16* __restrict__ shk,
    const u16* __restrict__ kf, const u16* __restrict__ ssmD, const u16* __restrict__ wg, const u16* __restrict__ wu,
    const u16* __restrict__ wd, const u16* __restrict__ cc, const u16* __restrict__ wmod, char* __restrict__ ws,
    u16* __restrict__ out) {
  extern __shared__ __attribute__((aligned(16))) char sm[];
  const int b = blockIdx.x, t = threadIdx.x;
  STAMP_DECL
  STAMP(0);
  const int w = __builtin_amdgcn_readfirstlane(t >> 6), lane = t & 63;
  const int xcd = b & 7, bi = b >> 3;
  const auto wsr = __builtin_amdgcn_make_buffer_rsrc(ws, 0, WS_END, 0x00020000);
  const int p = xcd * 32 + bi;
  const bool has_w = bi < 28;
  const int it = xcd * 28 + (has_w ? bi : 0);
  const int slab = it >> 3, kc = it & 7;
  const int rbk = (xcd & 1) * 8 + (bi & 7);
  const int cbk = (xcd >> 1) * 4 + (bi >> 3);
  const int sbk = xcd * 4 + (bi & 3);
  const int nbk = bi >> 2;
  const int g = lane >> 4, h = lane >> 5, qq = (lane & 15) >> 2, pp = lane & 3, l15 = lane & 15;

  u32 tag;
  u32x4 ka[2], kb[2];
  {
    const int sb = bi, db = xcd;
    const int rp = t >> 3, ci = t & 7;
    const int s0 = 64 * sb + 2 * rp, ch0 = 64 * db + 8 * ci;
    const u32 ep = BLD32(0, WS_EPOCH);
    u32x4 kr[5], vr[2], wr[4], qr[2];
#pragma unroll
    for (int i = 0; i < 5; ++i) {
      int s = s0 - 3 + i;
      kr[i] = *(const u32x4*)(k + (size_t)(s < 0 ? 0 : s) * D + ch0);
    }
#pragma unroll
    for (int i = 0; i < 2; ++i) vr[i] = *(const u32x4*)(v + (size_t)(s0 + i) * D + ch0);
#pragma unroll
    for (int i = 0; i < 4; ++i) wr[i] = *(const u32x4*)(shk + (size_t)ch0 * 4 + i * 8);
#pragma unroll
    for (int i = 0; i < 2; ++i) qr[i] = *(const u32x4*)(q + (size_t)(s0 + i) * D + ch0);
    u32 cpf;
    {
      u64 c0;
      int r0_;
      asm volatile("s_getpc_b64 %0\n\ts_mov_b32 %1, _Z5k_endPc@rel32@lo+4" : "=s"(c0), "=s"(r0_));
      const unsigned off = (unsigned)(bi * 20 + (t & 31)) * 64;
      const char* cp_ = (const char*)(c0 & ~63ull) + (off + 64 <= (unsigned)r0_ ? off : 0u);
      asm volatile("global_load_dword %0, %1, off" : "=v"(cpf) : "v"(cp_) : "memory");
    }
    SCHED_FENCE();
    tag = __builtin_amdgcn_readfirstlane(ep) + 1u;
#pragma unroll
    for (int i = 0; i < 3; ++i) {
      const bool neg = (s0 - 3 + i) < 0;
#pragma unroll
      for (int j = 0; j < 4; ++j) kr[i][j] = neg ? 0u : kr[i][j];
    }
    u32* Tu = (u32*)(sm + L_TU);
    u32* Tq = (u32*)(sm + L_TQ);
    const int x = 31 - rp;
    asm volatile("s_waitcnt vmcnt(0)" : "+v"(cpf), "+v"(wr[3]), "+v"(vr[1]), "+v"(kr[4]), "+v"(qr[1])::"memory");
    SCHED_FENCE();
    u32x4 xr[2], wv[4];
    const int r0 = 8 * b + 2 * w;
    const int mi = b < 192 ? b : 0;
    const int mslab = mi % 48, mq = mi / 48;
#pragma unroll
    for (int i = 0; i < 4; ++i)
      wv[i] = *(const u32x4*)(wmod + (size_t)(128 * mq + 4 * rp + i) * (6 * D) + 64 * mslab + 8 * ci);
    const u32x2 cv = *(const u32x2*)(cc + 128 * mq + 4 * rp);
#pragma unroll
    for (int ch = 0; ch < 2; ++ch) {
      const u16* kfr = kf + (size_t)(2 * p + ch) * S;
      ka[ch] = *(const u32x4*)(kfr + (t > 0 ? t - 1 : 0) * 8);
      kb[ch] = *(const u32x4*)(kfr + t * 8);
    }
#pragma unroll
    for (int i = 0; i < 2; ++i) xr[i] = *(const u32x4*)(q + (size_t)(r0 + i) * D + lane * 8);
    SCHED_FENCE();
#pragma unroll
    for (int j = 0; j < 4; ++j) {
      const int xs = 2 * rp;
      u32x2 val = {qr[0][j], qr[1][j]};
      *(u32x2*)&Tq[(4 * ci + j) * 64 + ((((xs >> 2) ^ (2 * ci)) << 2) | (xs & 3))] = val;
    }
#pragma unroll
    for (int j = 0; j < 8; ++j) {
      u32 w01 = wr[j >> 1][(j & 1) * 2], w23 = wr[j >> 1][(j & 1) * 2 + 1];
      float w0 = lo2f(w01), w1 = hi2f(w01), w2 = lo2f(w23), w3 = hi2f(w23);
      float kk[5];
#pragma unroll
      for (int i = 0; i < 5; ++i) kk[i] = (j & 1) ? hi2f(kr[i][j >> 1]) : lo2f(kr[i][j >> 1]);
      u32 pv = pk2(kk[0] * w3, kk[1] * w3);
      float a0 = lo2f(pv), a1 = hi2f(pv);
      pv = pk2(kk[1] * w2, kk[2] * w2);
      pv = pk2(a0 + lo2f(pv), a1 + hi2f(pv));
      a0 = lo2f(pv); a1 = hi2f(pv);
      pv = pk2(kk[2] * w1, kk[3] * w1);
      pv = pk2(a0 + lo2f(pv), a1 + hi2f(pv));
      a0 = lo2f(pv); a1 = hi2f(pv);
      pv = pk2(kk[3] * w0, kk[4] * w0);
      pv = pk2(a0 + lo2f(pv), a1 + hi2f(pv));
      a0 = lo2f(pv); a1 = hi2f(pv);
      float v0 = (j & 1) ? hi2f(vr[0][j >> 1]) : lo2f(vr[0][j >> 1]);
      float v1 = (j & 1) ? hi2f(vr[1][j >> 1]) : lo2f(vr[1][j >> 1]);
      u32 u = pk2(a1 * v1, a0 * v0);
      Tu[(8 * ci + j) * 32 + ((((x >> 2) ^ ci) << 2) | (x & 3))] = u;
    }
    STAMP(1);
    RAWBAR();
    {
      const int tbase = S - 64 * (sb + 1);
#pragma unroll
      for (int i = 0; i < 2; ++i) {
        const int idx = t + 256 * i;
        const int ch = idx >> 3, c8 = idx & 7;
        u32x4 val = *(const u32x4*)&Tu[ch * 32 + ((c8 ^ (ch >> 3)) << 2)];
        BST128(val, ((64 * db + ch) * S + tbase + c8 * 8) * 2, WS_URT);
        const int pr = idx >> 4, c16 = idx & 15;
        u32x4 qv = *(const u32x4*)&Tq[pr * 64 + ((c16 ^ (2 * (pr >> 2))) << 2)];
        BST128(qv, ((32 * db + pr) * S + 64 * sb + c16 * 4) * 4, WS_QT2);
      }
    }
    SCHED_FENCE();
    {
      float acc[8];
#pragma unroll
      for (int j = 0; j < 8; ++j) acc[j] = 0.f;
#pragma unroll
      for (int i = 0; i < 4; ++i) {
        float xx = (i & 1) ? hi2f(cv[i >> 1]) : lo2f(cv[i >> 1]);
        float sl = rb(xx / (1.f + expf(-xx)));
#pragma unroll
        for (int j = 0; j < 4; ++j) {
          acc[2 * j] += sl * lo2f(wv[i][j]);
          acc[2 * j + 1] += sl * hi2f(wv[i][j]);
        }
      }
#pragma unroll
      for (int j = 0; j < 8; ++j) acc[j] = red_8_16_32(acc[j]);
      if (lane < 8) {
        float* red = (float*)(sm + L_SCR) + w * 64 + lane * 8;
        *(f32x4*)red = (f32x4){acc[0], acc[1], acc[2], acc[3]};
        *(f32x4*)(red + 4) = (f32x4){acc[4], acc[5], acc[6], acc[7]};
      }
    }
    DRAINBAR();
    if (t == 0) BST32(tag, b * 64, WS_FLA);
    if (w == 1 && b < 192) {
      const float* red = (const float*)(sm + L_SCR);
      const float r = (red[lane] + red[64 + lane]) + (red[128 + lane] + red[192 + lane]);
      u32x2 gr = {__float_as_uint(r), tag};
      BST64(gr, (mq * 3072 + 64 * mslab + lane) * 8, WS_MODG);
    }
    {
      float mean[2], var[2];
#pragma unroll
      for (int i = 0; i < 2; ++i) {
        float a = 0.f;
#pragma unroll
        for (int j = 0; j < 4; ++j) a = dot2(xr[i][j], 0x3f803f80u, a);
        mean[i] = wave_sum(a) * (1.f / D);
      }
#pragma unroll
      for (int i = 0; i < 2; ++i) {
        float a = 0.f;
#pragma unroll
        for (int j = 0; j < 4; ++j) {
          float d0 = lo2f(xr[i][j]) - mean[i], d1 = hi2f(xr[i][j]) - mean[i];
          a += d0 * d0 + d1 * d1;
        }
        var[i] = wave_sum(a) * (1.f / D);
      }
      const float m_ = lane == 0 ? mean[0] : mean[1], v_ = lane == 0 ? var[0] : var[1];
      const float sd = __builtin_amdgcn_sqrtf(rb(rb(v_) + 1e-5f));
      u32x2 gr = {pk2(m_, sd), tag};
      if (lane < 2) BST64(gr, (r0 + lane) * 8, WS_ST1G);
    }
  }
  STAMP(2);

  float gbv[16];
  {
    u32x4 wl[6];
    {
      const unsigned wo1 = ((unsigned)(16 * w + (lane >> 3)) * F + 8 * (lane & 7)) * 2;
      const unsigned wo2 = ((unsigned)(16 * w + (lane >> 3)) * D + 8 * (lane & 7)) * 2;
      const char* wgb = (const char*)(wg + (size_t)(64 * kc) * F + 64 * slab);
      const char* wub = (const char*)(wu + (size_t)(64 * kc) * F + 64 * slab);
      const char* wdb = (const char*)(wd + (size_t)(64 * slab) * D + 64 * kc);
      wl[0] = *(const u32x4*)(wgb + wo1);
      wl[1] = *(const u32x4*)(wgb + 8 * F * 2 + wo1);
      wl[2] = *(const u32x4*)(wub + wo1);
      wl[3] = *(const u32x4*)(wub + 8 * F * 2 + wo1);
      wl[4] = *(const u32x4*)(wdb + wo2);
      wl[5] = *(const u32x4*)(wdb + 8 * D * 2 + wo2);
    }
    const u32 dpair = ((const u32*)ssmD)[p];
    SCHED_FENCE();
#pragma unroll
    for (int ch = 0; ch < 2; ++ch) {
      u32 wv[8] = {ka[ch][0], ka[ch][1], ka[ch][2], ka[ch][3], kb[ch][0], kb[ch][1], kb[ch][2], kb[ch][3]};
#pragma unroll
      for (int i = 0; i < 4; ++i) wv[i] = t > 0 ? wv[i] : 0u;
      u32 od[7];
#pragma unroll
      for (int i = 0; i < 7; ++i) od[i] = __builtin_amdgcn_alignbit(wv[i + 1], wv[i], 16);
      char* base = sm + L_FC + ch * (8 * L_CS) + (t + 1) * 16;
#pragma unroll
      for (int r = 0; r < 8; ++r) {
        const int sg = r + 1;
        u32x4 val;
        if (sg & 1) {
          const int k0 = (sg - 1) / 2;
          val = (u32x4){od[k0], od[k0 + 1], od[k0 + 2], od[k0 + 3]};
        } else {
          const int k0 = sg / 2;
          val = (u32x4){wv[k0], wv[k0 + 1], wv[k0 + 2], wv[k0 + 3]};
        }
        *(u32x4*)(base + r * L_CS) = val;
      }
    }
    {
      const u32x4 z = {0, 0, 0, 0};
      if (t < 16) *(u32x4*)(sm + L_FC + t * L_CS) = z;
      if (t >= 64 && t < 128) *(u32x4*)(sm + L_U + ((t >> 5) & 1) * 4608 + 4096 + (t & 31) * 16) = z;
      if (t == 128) *(u32*)(sm + L_SCR + 2048) = 0u;
    }
    STAMP(3);
    {
      const int e = t < 64 ? 4 * D + 64 * kc + t
                  : t < 128 ? 3 * D + 64 * kc + (t - 64)
                  : t < 192 ? 5 * D + 64 * nbk + (t - 128)
                            : (((t - 192) >> 1) % 3) * D + 2 * p + (t & 1);
      float val = 0.f;
      for (int spins = 0; spins < SPIN_CAP; ++spins) {
        u32x2 g0 = BLD64(e * 8, WS_MODG), g1 = BLD64(e * 8, WS_MODG + 3072 * 8), g2 = BLD64(e * 8, WS_MODG + 2 * 3072 * 8),
              g3 = BLD64(e * 8, WS_MODG + 3 * 3072 * 8);
        val = (__uint_as_float(g0[0]) + __uint_as_float(g1[0])) + (__uint_as_float(g2[0]) + __uint_as_float(g3[0]));
        if (__all(g0[1] == tag && g1[1] == tag && g2[1] == tag && g3[1] == tag)) break;
        __builtin_amdgcn_s_sleep(2);
      }
      val = rb(val);
      if (t < 64 || (t >= 194 && t < 196)) val = rb(1.f + val);
      ((float*)(sm + L_SCR + 1024))[t] = val;
    }
    STAMP(4);
    if (w == 0) {
      for (int spins = 0; spins < SPIN_CAP; ++spins) {
        const u32 f = BLD32(((lane & 31) * 8 + xcd) * 64, WS_FLA);
        if (__all(f == tag)) break;
        __builtin_amdgcn_s_sleep(1);
      }
    }
    RAWBAR();
    STAMP(5);
    u32x4 uu[2], qp[2], sg[4];
#pragma unroll
    for (int ch = 0; ch < 2; ++ch) uu[ch] = BLD128(((2 * p + ch) * S + t * 8) * 2, WS_URT);
#pragma unroll
    for (int i = 0; i < 2; ++i) qp[i] = BLD128((p * S + 1024 * i + 4 * t) * 4, WS_QT2);
    SCHED_FENCE();
    const float* mv = (const float*)(sm + L_SCR + 1024);
    {
      char* wst = sm + L_WST + w * L_WSTW;
      {
        const int row = lane >> 3, c16 = lane & 7;
        char* d1 = wst + row * 288 + c16 * 16;
        *(u32x4*)(d1) = wl[0];
        *(u32x4*)(d1 + 8 * 288) = wl[1];
        *(u32x4*)(d1 + 128) = wl[2];
        *(u32x4*)(d1 + 8 * 288 + 128) = wl[3];
        char* d2 = wst + 16 * 288 + row * 144 + c16 * 16;
        *(u32x4*)(d2) = wl[4];
        *(u32x4*)(d2 + 8 * 144) = wl[5];
      }
      __builtin_amdgcn_fence(__ATOMIC_RELEASE, "wavefront");
      __builtin_amdgcn_wave_barrier();
      __builtin_amdgcn_fence(__ATOMIC_ACQUIRE, "wavefront");
      const f32x4 sc0 = *(const f32x4*)(mv + 16 * w + 8 * h);
      const f32x4 sc1 = *(const f32x4*)(mv + 16 * w + 8 * h + 4);
      const f32x4 sb0 = *(const f32x4*)(mv + 64 + 16 * w + 8 * h);
      const f32x4 sb1 = *(const f32x4*)(mv + 64 + 16 * w + 8 * h + 4);
      const u32 sbv[4] = {pk2(sb0[0], sb0[1]), pk2(sb0[2], sb0[3]), pk2(sb1[0], sb1[1]), pk2(sb1[2], sb1[3])};
      const int ks = 4 * kc + w;
      const unsigned oob = has_w ? 0u : 0x80000000u;
#pragma unroll
      for (int j = 0; j < 4; ++j) {
        const int mtg = 4 * slab + j;
        const char* a0 = wst + (8 * h + qq) * 288 + (g & 1) * 128 + j * 32 + pp * 8;
        s16x4 lo = __builtin_amdgcn_ds_read_tr16_b64_v4i16((lds_s4p)(a0));
        s16x4 hi = __builtin_amdgcn_ds_read_tr16_b64_v4i16((lds_s4p)(a0 + 4 * 288));
        u32x2 l2 = __builtin_bit_cast(u32x2, lo), h2 = __builtin_bit_cast(u32x2, hi);
        u32 wv[4] = {l2[0], l2[1], h2[0], h2[1]};
        u32x4 o;
        o[0] = pk2(lo2f(wv[0]) * sc0[0], hi2f(wv[0]) * sc0[1]);
        o[1] = pk2(lo2f(wv[1]) * sc0[2], hi2f(wv[1]) * sc0[3]);
        o[2] = pk2(lo2f(wv[2]) * sc1[0], hi2f(wv[2]) * sc1[1]);
        o[3] = pk2(lo2f(wv[3]) * sc1[2], hi2f(wv[3]) * sc1[3]);
        float cs = 0.f, bi2 = 0.f;
#pragma unroll
        for (int e = 0; e < 4; ++e) {
          cs = dot2(o[e], 0x3f803f80u, cs);
          bi2 = dot2(wv[e], sbv[e], bi2);
        }
        BST128(o, (lane * 16) | oob, WS_W1F + (mtg / 7) * W1F_CB + ks * 7168 + (mtg % 7) * 1024);
        f32x2 r = {cs, bi2};
        *(f32x2*)(sm + L_RED + ((w * 4 + j) * 64 + lane) * 8) = r;
      }
#pragma unroll
      for (int j = 0; j < 2; ++j) {
        const char* a0 = wst + 16 * 288 + (4 * h + qq) * 144 + j * 64 + (g & 1) * 32 + pp * 8;
        s16x4 lo = __builtin_amdgcn_ds_read_tr16_b64_v4i16((lds_s4p)(a0));
        s16x4 hi = __builtin_amdgcn_ds_read_tr16_b64_v4i16((lds_s4p)(a0 + 8 * 144));
        u32x2 l2 = __builtin_bit_cast(u32x2, lo), h2 = __builtin_bit_cast(u32x2, hi);
        u32x4 o = {l2[0], l2[1], h2[0], h2[1]};
        BST128(o, (lane * 16) | oob, WS_WDF + ((2 * kc + j) * 112 + 4 * slab + w) * 1024);
      }
    }
    SCHED_FENCE();
#pragma unroll
    for (int ch = 0; ch < 2; ++ch) *(u32x4*)(sm + L_U + ch * 4608 + t * 16) = uu[ch];
    const int cch = w >> 1, c0 = 4 * (w & 1);
    const char* fb = sm + L_FC + cch * (8 * L_CS) + (l15 & 7) * L_CS + 2 * ((l15 & 8) + 8 * g) + 64 * c0;
    const char* gb = sm + L_U + cch * 4608 + 2 * (2032 - 256 * 7 - 16 * l15 + 8 * g) + 64 * c0;
    u32x4 df;
    {
      const u32 dv = c0 == 0 ? (cch ? (dpair >> 16) : (dpair & 0xffffu)) : 0u;
      const int kap = 15 - l15 - 8 * g;
#pragma unroll
      for (int j = 0; j < 4; ++j) df[j] = (kap == 2 * j) ? dv : (kap == 2 * j + 1) ? (dv << 16) : 0u;
    }
    asm volatile("" : "+v"(df));
    STAMP(6);
    RAWBAR();
    {
      const f32x2* red = (const f32x2*)(sm + L_RED);
      f32x2 rr = (red[(0 * 4 + w) * 64 + lane] + red[(1 * 4 + w) * 64 + lane]) +
                 (red[(2 * 4 + w) * 64 + lane] + red[(3 * 4 + w) * 64 + lane]);
      auto x0 = __builtin_amdgcn_permlane32_swap(__float_as_uint(rr[0]), __float_as_uint(rr[0]), false, false);
      auto x1 = __builtin_amdgcn_permlane32_swap(__float_as_uint(rr[1]), __float_as_uint(rr[1]), false, false);
      u32x2 o = {__float_as_uint(__uint_as_float(x0[0]) + __uint_as_float(x0[1])),
                 __float_as_uint(__uint_as_float(x1[0]) + __uint_as_float(x1[1]))};
      const int mtg = 4 * slab + w;
      if (has_w && lane < 32) BST64(o, lane * 8, WS_CSP + (((mtg / 7) * 8 + kc) * 224 + (mtg % 7) * 32) * 8);
    }
#pragma unroll
    for (int i = 0; i < 2; ++i) {
      sg[2 * i] = BLD128((1024 * i + 4 * t) * 8, WS_ST1G);
      sg[2 * i + 1] = BLD128((1024 * i + 4 * t) * 8 + 16, WS_ST1G);
    }
    SCHED_FENCE();
    f32x4 acc[8];
    {
      bf16x8 Fr[2][8], Gr[2][8];
#pragma unroll
      for (int n = 0; n < 8; ++n) Gr[0][n] = *(const bf16x8*)(gb + 512 * (7 - n));
#pragma unroll
      for (int n = 0; n < 8; ++n) Fr[0][n] = *(const bf16x8*)(fb + 512 * n);
      SCHED_FENCE();
#pragma unroll
      for (int m = 0; m < 8; ++m)
        acc[m] = __builtin_amdgcn_mfma_f32_16x16x32_bf16(__builtin_bit_cast(bf16x8, df), Gr[0][m],
                                                         (f32x4){0.f, 0.f, 0.f, 0.f}, 0, 0, 0);
      SCHED_FENCE();
#pragma unroll
      for (int c = 0; c < 4; ++c) {
        const int cur = c & 1, nxt = cur ^ 1;
#pragma unroll
        for (int i = 0; i < 36; ++i) {
          const int n = conv_n(i), dl = conv_d(i);
          acc[n + dl] = __builtin_amdgcn_mfma_f32_16x16x32_bf16(Fr[cur][n], Gr[cur][dl], acc[n + dl], 0, 0, 0);
          if (c < 3 && (i & 1) && i < 32) {
            const int j = i >> 1;
            if (j == 0) Fr[nxt][0] = *(const bf16x8*)(fb + 64 * (c + 1));
            else if (j <= 8) Gr[nxt][j - 1] = *(const bf16x8*)(gb + 512 * (8 - j) + 64 * (c + 1));
            else Fr[nxt][j - 8] = *(const bf16x8*)(fb + 512 * (j - 8) + 64 * (c + 1));
          }
          if (i & 1) SCHED_FENCE();
        }
      }
    }
    {
      float* part = (float*)(sm + L_PART) + w * (8 * 288) + g * 72 + l15 * 4;
#pragma unroll
      for (int m = 0; m < 8; ++m) *(f32x4*)(part + m * 288) = acc[m];
    }
    STAMP(7);
    for (int spins = 0; spins < SPIN_CAP; ++spins) {
      bool ok = true;
#pragma unroll
      for (int i = 0; i < 4; ++i) ok = ok && sg[i][1] == tag && sg[i][3] == tag;
      if (__all(ok)) break;
      __builtin_amdgcn_s_sleep(2);
#pragma unroll
      for (int i = 0; i < 2; ++i) {
        sg[2 * i] = BLD128((1024 * i + 4 * t) * 8, WS_ST1G);
        sg[2 * i + 1] = BLD128((1024 * i + 4 * t) * 8 + 16, WS_ST1G);
      }
    }
    const float sa0 = mv[192], sa1 = mv[193], oc0 = mv[194], oc1 = mv[195], ga0 = mv[196], ga1 = mv[197];
    {
      const int fn = w >> 1;
#pragma unroll
      for (int i = 0; i < 4; ++i) {
        const f32x4 gq = *(const f32x4*)(mv + 128 + 32 * fn + 8 * h + (i >> 1) * 16 + (i & 1) * 4);
#pragma unroll
        for (int e = 0; e < 4; ++e) gbv[4 * i + e] = gq[e];
      }
    }
    float h0[8], h1[8];
#pragma unroll
    for (int i = 0; i < 8; ++i) {
      const u32 qv = qp[i >> 2][i & 3];
      const u32 ms = sg[i >> 1][(i & 1) * 2];
      const float mb = lo2f(ms), rs = __builtin_amdgcn_rcpf(hi2f(ms));
      u32 x = pk2(lo2f(qv) - mb, hi2f(qv) - mb);
      x = pk2(lo2f(x) * rs, hi2f(x) * rs);
      x = pk2(lo2f(x) * oc0, hi2f(x) * oc1);
      x = pk2(lo2f(x) + sa0, hi2f(x) + sa1);
      h0[i] = lo2f(x);
      h1[i] = hi2f(x);
    }
    DRAINBAR();
    if (t == 0) BST32(tag, b * 64, WS_FLW);
    {
      const float* part = (const float*)(sm + L_PART) + (t >> 6) * 288 + (t & 3) * 72 + ((t >> 2) & 15) * 4;
#pragma unroll
      for (int i = 0; i < 2; ++i) {
        const f32x4 y0 = *(const f32x4*)(part + i * 4 * 288) + *(const f32x4*)(part + 2304 + i * 4 * 288);
        const f32x4 y1 = *(const f32x4*)(part + 2 * 2304 + i * 4 * 288) + *(const f32x4*)(part + 3 * 2304 + i * 4 * 288);
        u32x4 o;
#pragma unroll
        for (int e = 0; e < 4; ++e) {
          const u32 z = pk2(y0[e], y1[e]);
          u32 a = pk2(h0[4 * i + e] * lo2f(z), h1[4 * i + e] * hi2f(z));
          a = pk2(ga0 * lo2f(a), ga1 * hi2f(a));
          o[e] = pk2(lo2f(qp[i][e]) + lo2f(a), hi2f(qp[i][e]) + hi2f(a));
        }
        BST128(o, (p * S + 1024 * i + 4 * t) * 4, WS_X1P);
      }
    }
    DRAINBAR();
    if (t == 0) BST32(tag, b * 64, WS_FL1);
  }
  STAMP(8);

  const int st = rbk * 4 + w;
  const char* wsrc = ws + WS_W1F + (size_t)cbk * W1F_CB + w * 7168;
  const char* asrc = ws + WS_X1P + (size_t)(8 * w) * (S * 4) + rbk * 512;
  const char* dsrc = ws + WS_WDF + ((size_t)(2 * nbk) * 112 + 28 * w) * 1024;
  const unsigned lbase = (unsigned)(uintptr_t)(__attribute__((address_space(3))) char*)sm;
  const unsigned ldw = lbase + w * 7168;
  const unsigned lda = lbase + L_A + w * 4096;
  const unsigned voff = lane * 16;
  const unsigned aoff = (lane >> 5) * (S * 4) + (lane & 31) * 16;

#define VW(g, j)                                                                                  \
  {                                                                                               \
    if ((j) == 0) GDMA(voff, wsrc + (g) * G_W, ldw + ((g) & 3) * G_W, 0);                         \
    if ((j) == 1) GDMA(voff, wsrc + (g) * G_W, ldw + ((g) & 3) * G_W, 1024);                      \
    if ((j) == 2) GDMA(voff, wsrc + (g) * G_W, ldw + ((g) & 3) * G_W, 2048);                      \
    if ((j) == 3) GDMA(voff, wsrc + (g) * G_W, ldw + ((g) & 3) * G_W, 3072);                      \
    if ((j) == 4) GDMA(voff, wsrc + (g) * G_W + 4096, ldw + ((g) & 3) * G_W + 4096, 0);           \
    if ((j) == 5) GDMA(voff, wsrc + (g) * G_W + 4096, ldw + ((g) & 3) * G_W + 4096, 1024);        \
    if ((j) == 6) GDMA(voff, wsrc + (g) * G_W + 4096, ldw + ((g) & 3) * G_W + 4096, 2048);        \
  }
#define VA(g, j) GDMA(aoff, asrc + (size_t)((g) * 32 + 2 * (j)) * (S * 4), lda + ((g) % 3) * G_A + (j) * 1024, 0);
#define VWALL(g) { VW(g, 0) VW(g, 1) VW(g, 2) VW(g, 3) VW(g, 4) VW(g, 5) VW(g, 6) }
#define VAALL(g) { VA(g, 0) VA(g, 1) VA(g, 2) VA(g, 3) }
#define WDM(q_) { if ((q_) >= 0 && (q_) < 20) GDMA(voff, dsrc + ((q_) & 1) * (112 * 1024) + ((q_) >> 1) * 1024, lbase + wd_lds_off((q_) < 0 ? 0 : (q_), w), 0); }

  for (int spins = 0; spins < SPIN_CAP; ++spins) {
    const u32 f = BLD32(t * 64, WS_FLW);
    if (__all(f == tag)) break;
    __builtin_amdgcn_s_sleep(1);
  }
  RAWBAR();
  VWALL(0) VWALL(1)
  STAMP(9);
  for (int spins = 0; spins < SPIN_CAP; ++spins) {
    const u32 f = BLD32(t * 64, WS_FL1);
    if (__all(f == tag)) break;
    __builtin_amdgcn_s_sleep(1);
  }
  RAWBAR();
  STAMP(10);
  if (b == 0 && t == 0) BST32(tag, 0, WS_EPOCH);
  VAALL(0)
  u32x4 sx[2];
  {
    const char* sbase = ws + WS_X1P + (size_t)(rbk * 128 + cbk * 8) * 4;
    const unsigned so = (unsigned)(t >> 1) * (S * 4) + (t & 1) * 16;
    asm volatile("global_load_dwordx4 %0, %1, %2 sc1" : "=v"(sx[0]) : "v"(so), "s"(sbase) : "memory");
    asm volatile("global_load_dwordx4 %0, %1, %2 sc1" : "=v"(sx[1]) : "v"(so), "s"(sbase + (size_t)128 * (S * 4)) : "memory");
  }
  VAALL(1)
  VWALL(2)

  f32x16 acc[7];
#pragma unroll
  for (int m = 0; m < 7; ++m)
#pragma unroll
    for (int r = 0; r < 16; ++r) acc[m][r] = 0.f;
  const char* lsrc = sm + lane * 16;
  const char* lsa = sm + L_A + (4 * h * 128 + 32 * w + (lane & 31)) * 4;
  bf16x8 wf[2][7];
  u32x4 af[2];

#define WAITG(n)                                                              \
  SCHED_FENCE();                                                              \
  asm volatile("s_waitcnt vmcnt(" #n ") lgkmcnt(0)" ::: "memory");            \
  __builtin_amdgcn_s_barrier();                                               \
  asm volatile("" ::: "memory");                                              \
  SCHED_FENCE();
#define KOFF(i) ((((i) >> 2) & 3) * G_W + ((i) & 3) * 7168)
#define AOFF(i) ((((i) >> 2) % 3) * G_A + ((i) & 3) * 4096)
#define RD(i, m) wf[(i) & 1][m] = *(const bf16x8*)(lsrc + KOFF(i) + (m) * 1024);
#define RA(dst, i)                                                                   \
  {                                                                                  \
    dst[0] = *(const u32*)(lsa + AOFF(i));                                           \
    dst[1] = *(const u32*)(lsa + AOFF(i) + 512);                                     \
    dst[2] = *(const u32*)(lsa + AOFF(i) + 1024);                                    \
    dst[3] = *(const u32*)(lsa + AOFF(i) + 1536);                                    \
  }
#define MM(i, m) acc[m] = __builtin_amdgcn_mfma_f32_32x32x16_bf16(wf[(i) & 1][m], __builtin_bit_cast(bf16x8, af[(i) & 1]), acc[m], 0, 0, 0);
#define VX(ga, gw, q0, j)                                                                        \
  {                                                                                              \
    if ((ga) >= 0 && (j) < 4) VA((ga) < 0 ? 0 : (ga), (j) & 3)                                   \
    if ((gw) >= 0 && (j) >= 4 && (j) < 11) VW((gw) < 0 ? 0 : (gw), (j) - 4)                      \
    if ((q0) >= 0 && (j) >= 4 && (j) < 11) WDM((q0) + (j) - 4)                                   \
  }
#define STEP(i, ga, gw, q0, j0)                                                        \
  {                                                                                    \
    MM(i, 0) RA(af[((i) + 1) & 1], (i) + 1) SCHED_FENCE();                             \
    MM(i, 1) RD((i) + 1, 0) RD((i) + 1, 1) SCHED_FENCE();                              \
    MM(i, 2) RD((i) + 1, 2) RD((i) + 1, 3) SCHED_FENCE();                              \
    MM(i, 3) RD((i) + 1, 4) RD((i) + 1, 5) SCHED_FENCE();                              \
    MM(i, 4) RD((i) + 1, 6) VX(ga, gw, q0, j0) SCHED_FENCE();                          \
    MM(i, 5) VX(ga, gw, q0, (j0) + 1) SCHED_FENCE();                                   \
    MM(i, 6) VX(ga, gw, q0, (j0) + 2) SCHED_FENCE();                                   \
  }
#define STEP4(i, ga, gw, q0) STEP(i, ga, gw, q0, 0) STEP((i) + 1, ga, gw, q0, 3) STEP((i) + 2, ga, gw, q0, 6) STEP((i) + 3, ga, gw, q0, 9)
  SCHED_FENCE();
  asm volatile("s_waitcnt vmcnt(11)" : "+v"(sx[0]), "+v"(sx[1])::"memory");
  {
    float sv[8];
#pragma unroll
    for (int e = 0; e < 4; ++e) {
      sv[e] = red_2_64(dot2(sx[1][e], 0x3f803f80u, dot2(sx[0][e], 0x3f803f80u, 0.f)));
      sv[4 + e] = red_2_64(dot2(sx[1][e], sx[1][e], dot2(sx[0][e], sx[0][e], 0.f)));
    }
    if (lane < 2) {
      float* ps = (float*)(sm + L_A + 2 * G_A) + (w * 2 + lane) * 8;
      *(f32x4*)ps = (f32x4){sv[0], sv[1], sv[2], sv[3]};
      *(f32x4*)(ps + 4) = (f32x4){sv[4], sv[5], sv[6], sv[7]};
    }
  }
  WAITG(11)
  STAMP(11);
  RA(af[0], 0)
  RD(0, 0) RD(0, 1) RD(0, 2) RD(0, 3) RD(0, 4) RD(0, 5) RD(0, 6)
  if (w == 0) {
    const float* ps = (const float*)(sm + L_A + 2 * G_A) + ((lane >> 2) & 1) * 8 + (lane & 3);
    const float s1 = (ps[0] + ps[16]) + (ps[32] + ps[48]);
    const float s2 = (ps[4] + ps[20]) + (ps[36] + ps[52]);
    const float mean = s1 * (1.f / D);
    const float var = fmaxf(s2 * (1.f / D) - mean * mean, 0.f);
    const float sdv = __builtin_amdgcn_sqrtf(rb(rb(var) + 1e-5f));
    u32x2 gr = {pk2(mean, sdv), tag};
    if (lane < 8) BST64(gr, (rbk * 128 + cbk * 8 + lane) * 8, WS_ST2G);
    asm volatile("s_waitcnt lgkmcnt(0)" ::: "memory");
  }
  SCHED_FENCE();
  VX(2, 3, -1, 0) VX(2, 3, -1, 1)
  STEP(0, 2, 3, -1, 2) STEP(1, 2, 3, -1, 5) STEP(2, 2, 3, -1, 8)
  WAITG(18)
  STEP4(3, 3, 4, -1)
  WAITG(18)
  STEP4(7, 4, 5, -1)
  WAITG(18)
  STEP4(11, 5, 6, -1)
  WAITG(18)
  STEP4(15, 6, 7, -1)
  u32x2 stg, cp[8];
  const char* stp = ws + WS_ST2G + (size_t)st * 256;
  const unsigned sto = (lane & 31) * 8;
  asm volatile("global_load_dwordx2 %0, %1, %2 sc1" : "=v"(stg) : "v"(sto), "s"(stp) : "memory");
  {
    const char* cbase = ws + WS_CSP + (size_t)cbk * (8 * 224 * 8);
    const unsigned co = (t < 224 ? t : 0) * 8;
#pragma unroll
    for (int i = 0; i < 8; ++i)
      asm volatile("global_load_dwordx2 %0, %1, %2 sc1" : "=v"(cp[i]) : "v"(co), "s"(cbase + i * 1792) : "memory");
  }
  WAITG(27)
  STEP4(19, 7, -1, 0)
  WAITG(11)
  asm volatile("" : "+v"(cp[0]), "+v"(cp[1]), "+v"(cp[2]), "+v"(cp[3]), "+v"(cp[4]), "+v"(cp[5]), "+v"(cp[6]), "+v"(cp[7]));
  {
    float cs = 0.f, bs = 0.f;
#pragma unroll
    for (int i = 0; i < 8; ++i) {
      cs += __uint_as_float(cp[i][0]);
      bs += __uint_as_float(cp[i][1]);
    }
    const float c0 = rb(cs), c1 = rb(cs - c0), c2 = rb(cs - c0 - c1);
    const float b0 = rb(bs), b1 = rb(bs - b0), b2 = rb(bs - b0 - b1);
    if (t < 224) {
      char* cd = sm + L_COR + (t >> 5) * 1024 + (t & 31) * 16;
      *(u32x4*)cd = (u32x4){pk2(c0, c1), pk2(c2, b0), pk2(b1, b2), 0u};
      *(u32x4*)(cd + 512) = (u32x4){0u, 0u, 0u, 0u};
    }
  }
  SCHED_FENCE();
  STEP4(23, -1, -1, 7)
  WAITG(14)
  asm volatile("" : "+v"(stg));
  for (int spins = 0; !__all(stg[1] == tag) && spins < SPIN_CAP; ++spins) {
    __builtin_amdgcn_s_sleep(1);
    asm volatile("global_load_dwordx2 %0, %1, %2 sc1\n\ts_waitcnt vmcnt(0)" : "=&v"(stg) : "v"(sto), "s"(stp) : "memory");
  }
  SCHED_FENCE();
  STEP(27, -1, -1, 14, 4)
  bf16x8 tl[2][4];
  u32x4 at[3];
#define RDT(m, k_) tl[(m) & 1][k_] = *(const bf16x8*)(lsrc + ((k_) == 3 ? L_COR : KOFF(29 + (k_))) + (m) * 1024);
  const float mb = lo2f(stg[0]), sd = hi2f(stg[0]);
  float rs2;
  {
    MM(28, 0) RA(at[0], 29) SCHED_FENCE();
    MM(28, 1) RDT(0, 0) RDT(0, 1) SCHED_FENCE();
    MM(28, 2) RA(at[1], 30) WDM(17) SCHED_FENCE();
    MM(28, 3) RDT(0, 2) RDT(0, 3) WDM(18) SCHED_FENCE();
    MM(28, 4) RA(at[2], 31) WDM(19) SCHED_FENCE();
    MM(28, 5)
    const float rs = __builtin_amdgcn_rcpf(sd);
    rs2 = rs * rs;
    SCHED_FENCE();
    MM(28, 6) SCHED_FENCE();
  }
  STAMP(12);
  const u32x4 cb4 = {pk2(-mb, -mb), pk2(-mb, sd), pk2(sd, sd), 0u};
  const unsigned hoff = WS_HHF + ((unsigned)st * 112 + cbk * 7) * 1024;
  u32x4 eo;
#define EPJ(m, j)                                                                     \
  {                                                                                   \
    float u0 = acc[m][8 + 2 * (j)], u1 = acc[m][8 + 2 * (j) + 1];                     \
    asm volatile("" : "+v"(u0), "+v"(u1));                                            \
    float e0 = relu(acc[m][2 * (j)]) * u0 * rs2;                                      \
    float e1 = relu(acc[m][2 * (j) + 1]) * u1 * rs2;                                  \
    eo[j] = pk2(e0, e1);                                                              \
  }
#define EST(m) BST128(eo, voff + (m) * 1024, hoff);
#define MT(m, k_, bop) acc[m] = __builtin_amdgcn_mfma_f32_32x32x16_bf16(tl[(m) & 1][k_], __builtin_bit_cast(bf16x8, bop), acc[m], 0, 0, 0);
#define TAIL(m)                                                                                      \
  {                                                                                                  \
    MT(m, 0, at[0]) SCHED_FENCE();                                                                   \
    if ((m) < 6) { RDT((m) + 1, 0) RDT((m) + 1, 1) }                                                 \
    SCHED_FENCE();                                                                                   \
    MT(m, 1, at[1]) SCHED_FENCE();                                                                   \
    if ((m) < 6) { RDT((m) + 1, 2) RDT((m) + 1, 3) }                                                 \
    if ((m) > 0) EPJ((m) > 0 ? (m) - 1 : 0, 0)                                                       \
    SCHED_FENCE();                                                                                   \
    MT(m, 2, at[2]) SCHED_FENCE();                                                                   \
    if ((m) > 0) { EPJ((m) > 0 ? (m) - 1 : 0, 1) EPJ((m) > 0 ? (m) - 1 : 0, 2) }                     \
    SCHED_FENCE();                                                                                   \
    MT(m, 3, cb4) SCHED_FENCE();                                                                     \
    if ((m) > 0) { EPJ((m) > 0 ? (m) - 1 : 0, 3) EST((m) > 0 ? (m) - 1 : 0) }                        \
    SCHED_FENCE();                                                                                   \
  }
  TAIL(0) TAIL(1) TAIL(2) TAIL(3) TAIL(4) TAIL(5) TAIL(6)
  EPJ(6, 0) EPJ(6, 1) EPJ(6, 2) EPJ(6, 3) EST(6)
  asm volatile("s_waitcnt vmcnt(0)" ::: "memory");
  if (lane == 0) BST32(tag, (st * 16 + cbk) * 64, WS_FL2);
  STAMP(13);
  SCHED_FENCE();

  u32x4 wr2[36];
  const unsigned dso = WS_WDF + ((unsigned)(2 * nbk) * 112 + 28 * w) * 1024;
#pragma unroll
  for (int r = 0; r < 36; ++r) wr2[r] = BLD128(voff, dso + (r & 1) * (112 * 1024) + (10 + (r >> 1)) * 1024);
  const int fn = w >> 1, fs = w & 1;
  const int ncol = 64 * nbk + 32 * fn;
  const int srow = 64 * sbk + 32 * fs + (lane & 31);
  u32 xr[8];
#pragma unroll
  for (int i = 0; i < 8; ++i)
    xr[i] = BLD32(((4 * h) * S + (lane & 31)) * 4, WS_X1P + (((ncol >> 1) + (i >> 2) * 8 + (i & 3)) * S + 64 * sbk + 32 * fs) * 4);
  SCHED_FENCE();
  for (int spins = 0; spins < SPIN_CAP; ++spins) {
    const u32 f = BLD32(((2 * sbk + (lane & 1)) * 16 + 4 * w + ((lane >> 1) & 3)) * 64, WS_FL2);
    if (__all(f == tag)) break;
    __builtin_amdgcn_s_sleep(1);
  }
  __builtin_amdgcn_fence(__ATOMIC_ACQUIRE, "wavefront");
  STAMP(14);
  SCHED_FENCE();
  const unsigned hs = WS_HHF + ((unsigned)(2 * sbk) * 112 + 28 * w) * 1024;
  constexpr int WIN = 16;
  u32x4 hb[WIN][2];
#define HLD(i)                                                  \
  {                                                             \
    hb[(i) % WIN][0] = BLD128(voff, hs + (i) * 1024);           \
    hb[(i) % WIN][1] = BLD128(voff, hs + (112 + (i)) * 1024);   \
  }
#pragma unroll
  for (int i = 0; i < WIN; ++i) HLD(i)
  SCHED_FENCE();
  f32x16 ac2[2][2];
#pragma unroll
  for (int a = 0; a < 2; ++a)
#pragma unroll
    for (int c = 0; c < 2; ++c)
#pragma unroll
      for (int r = 0; r < 16; ++r) ac2[a][c][r] = 0.f;
  bf16x8 wl2[2][2];
  wl2[0][0] = *(const bf16x8*)(lsrc + wd_lds_off(0, w));
  wl2[0][1] = *(const bf16x8*)(lsrc + wd_lds_off(1, w));
#pragma unroll
  for (int i = 0; i < 28; ++i) {
    if (i + 1 < 10) {
      wl2[(i + 1) & 1][0] = *(const bf16x8*)(lsrc + wd_lds_off(2 * (i + 1), w));
      wl2[(i + 1) & 1][1] = *(const bf16x8*)(lsrc + wd_lds_off(2 * (i + 1) + 1, w));
    }
#pragma unroll
    for (int a = 0; a < 2; ++a)
#pragma unroll
      for (int c = 0; c < 2; ++c)
        ac2[a][c] = __builtin_amdgcn_mfma_f32_32x32x16_bf16(
            i < 10 ? wl2[i & 1][a] : __builtin_bit_cast(bf16x8, wr2[i < 10 ? 0 : 2 * (i - 10) + a]),
            __builtin_bit_cast(bf16x8, hb[i % WIN][c]), ac2[a][c], 0, 0, 0);
    if (i + WIN < 28) HLD(i + WIN)
    SCHED_FENCE();
  }
#define PARK(w_, j) (((j) - 1) * G_W + (w_) * 7168)
#pragma unroll
  for (int a = 0; a < 2; ++a)
#pragma unroll
    for (int c = 0; c < 2; ++c) {
      const int tile = a * 2 + c;
      if (tile != w) {
        const int j = (tile - w) & 3;
        char* dst = sm + PARK(w, j) + lane * 16;
#pragma unroll
        for (int r = 0; r < 4; ++r)
          *(f32x4*)(dst + r * 1024) =
              (f32x4){ac2[a][c][4 * r], ac2[a][c][4 * r + 1], ac2[a][c][4 * r + 2], ac2[a][c][4 * r + 3]};
      }
    }
  RAWBAR();
  f32x16 fin;
  if (w == 0) fin = ac2[0][0];
  else if (w == 1) fin = ac2[0][1];
  else if (w == 2) fin = ac2[1][0];
  else fin = ac2[1][1];
#pragma unroll
  for (int o = 1; o < 4; ++o) {
    const int ow = (w + o) & 3;
    const char* srcp = sm + PARK(ow, 4 - o) + lane * 16;
#pragma unroll
    for (int r = 0; r < 4; ++r) {
      f32x4 vv = *(const f32x4*)(srcp + r * 1024);
#pragma unroll
      for (int e = 0; e < 4; ++e) fin[4 * r + e] += vv[e];
    }
  }
  u32 pkd[8];
#pragma unroll
  for (int j = 0; j < 8; ++j) pkd[j] = pk2(fin[2 * j], fin[2 * j + 1]);
  u16* orow = out + (size_t)srow * D + ncol + 8 * h;
#pragma unroll
  for (int kk = 0; kk < 2; ++kk) {
    auto r0 = __builtin_amdgcn_permlane32_swap(pkd[4 * kk], pkd[4 * kk + 2], false, false);
    auto r1 = __builtin_amdgcn_permlane32_swap(pkd[4 * kk + 1], pkd[4 * kk + 3], false, false);
    u32 m4[4] = {r0[0], r1[0], r0[1], r1[1]};
    u32x4 o;
#pragma unroll
    for (int j = 0; j < 4; ++j) {
      u32 a = pk2(gbv[8 * kk + 2 * j] * lo2f(m4[j]), gbv[8 * kk + 2 * j + 1] * hi2f(m4[j]));
      o[j] = pk2(lo2f(xr[4 * kk + j]) + lo2f(a), hi2f(xr[4 * kk + j]) + hi2f(a));
    }
    *(u32x4*)(orow + 16 * kk) = o;
  }
  STAMP(15);
  STAMP_FLUSH();
}

__global__ void k_end(char* ws) {
  if (ws == nullptr) __builtin_trap();
}

extern "C" void kernel_launch(void* const* d_in, const int* in_sizes, int n_in, void* d_out, int out_size,
                              void* d_ws, size_t ws_size, hipStream_t stream) {
  (void)hipFuncSetAttribute((const void*)mega, hipFuncAttributeMaxDynamicSharedMemorySize, LDS_BYTES);
  mega<<<256, 256, LDS_BYTES, stream>>>((const u16*)d_in[0], (const u16*)d_in[1], (const u16*)d_in[2],
                                        (const u16*)d_in[3], (const u16*)d_in[4], (const u16*)d_in[5],
                                        (const u16*)d_in[7], (const u16*)d_in[8], (const u16*)d_in[9],
                                        (const u16*)d_in[10], (const u16*)d_in[11], (char*)d_ws, (u16*)d_out);
}
```

```cpp
#include <hip/hip_runtime.h>
#include <stdint.h>

typedef unsigned short u16;
typedef unsigned int u32;
typedef unsigned long long u64;
typedef __attribute__((ext_vector_type(8))) short bf16x8;
typedef __attribute__((ext_vector_type(4))) short s16x4;
typedef __attribute__((ext_vector_type(4))) float f32x4;
typedef __attribute__((ext_vector_type(2))) float f32x2;
typedef __attribute__((ext_vector_type(16))) float f32x16;
typedef __attribute__((ext_vector_type(4))) unsigned u32x4;
typedef __attribute__((ext_vector_type(2))) unsigned u32x2;
typedef __attribute__((ext_vector_type(2))) __bf16 bf2;
typedef __attribute__((address_space(3))) s16x4* lds_s4p;

#define DI __device__ __forceinline__

constexpr int S = 2048, D = 512, F = 1792;

DI float lo2f(u32 x) { return __uint_as_float(x << 16); }
DI float hi2f(u32 x) { return __uint_as_float(x & 0xffff0000u); }
DI u32 pk2(float a, float b) {
  f32x2 v = {a, b};
  return __builtin_bit_cast(u32, __builtin_convertvector(v, bf2));
}
DI float rb(float a) { return __uint_as_float(pk2(0.f, a)); }
DI f32x2 rb2(f32x2 a) { return (f32x2){rb(a[0]), rb(a[1])}; }
DI f32x2 up2(u32 x) { return (f32x2){lo2f(x), hi2f(x)}; }
DI u32 pk2v(f32x2 a) { return pk2(a[0], a[1]); }
DI float dot2(u32 a, u32 b, float c) {
  return __builtin_amdgcn_fdot2_f32_bf16(__builtin_bit_cast(bf2, a), __builtin_bit_cast(bf2, b), c, false);
}
template <int CTRL>
DI float dpp_add(float x) {
  return x + __uint_as_float(__builtin_amdgcn_update_dpp(0u, __float_as_uint(x), CTRL, 0xf, 0xf, false));
}
DI float red_16_32(float x) {
  auto a = __builtin_amdgcn_permlane16_swap(__float_as_uint(x), __float_as_uint(x), false, false);
  x = __uint_as_float(a[0]) + __uint_as_float(a[1]);
  auto b = __builtin_amdgcn_permlane32_swap(__float_as_uint(x), __float_as_uint(x), false, false);
  return __uint_as_float(b[0]) + __uint_as_float(b[1]);
}
DI float red_8_16_32(float x) { return red_16_32(dpp_add<0x128>(x)); }
DI float red_2_64(float x) {
  x = dpp_add<0x4E>(x);
  x = dpp_add<0x124>(x);
  return red_8_16_32(x);
}
DI float wave_sum(float x) { return red_2_64(dpp_add<0xB1>(x)); }
DI float relu(float x) {
  float r;
  asm("v_max_f32 %0, 0, %1" : "=v"(r) : "v"(x));
  return r;
}

#define STAMP_DECL
#define STAMP(i)
#define STAMP_FLUSH()
#define SCHED_FENCE() __builtin_amdgcn_sched_barrier(0)
#define RAWBAR()                                          \
  do {                                                    \
    __builtin_amdgcn_sched_barrier(0);                    \
    asm volatile("s_waitcnt lgkmcnt(0)" ::: "memory");    \
    __builtin_amdgcn_s_barrier();                         \
    asm volatile("" ::: "memory");                        \
    __builtin_amdgcn_sched_barrier(0);                    \
  } while (0)
#define DRAINBAR()                                                 \
  do {                                                             \
    __builtin_amdgcn_sched_barrier(0);                             \
    asm volatile("s_waitcnt vmcnt(0) lgkmcnt(0)" ::: "memory");    \
    __builtin_amdgcn_s_barrier();                                  \
    asm volatile("" ::: "memory");                                 \
    __builtin_amdgcn_sched_barrier(0);                             \
  } while (0)

constexpr u32 WS_EPOCH = 0;
constexpr u32 WS_FLA = 4096;
constexpr u32 WS_FLW = WS_FLA + 256 * 64;
constexpr u32 WS_FL1 = WS_FLW + 256 * 64;
constexpr u32 WS_FL2 = WS_FL1 + 256 * 64;
constexpr u32 WS_FL3 = WS_FL2 + 1024 * 64;
constexpr u32 WS_MODG = WS_FL3 + 1024 * 64;
constexpr u32 WS_ST1G = WS_MODG + 4 * 3072 * 8;
constexpr u32 WS_ST2G = WS_ST1G + 2048 * 8;
constexpr u32 WS_CSP = WS_ST2G + 2048 * 8;
constexpr u32 WS_URT = 1u << 20;
constexpr u32 WS_QT2 = WS_URT + 2u * S * D;
constexpr u32 WS_X1P = WS_QT2 + 2u * S * D;
constexpr u32 WS_X1S = WS_X1P + 2u * S * D;
constexpr u32 WS_WDF = WS_X1S + 2u * S * D;
constexpr u32 WS_HHF = WS_WDF + 2u * F * D;
constexpr u32 WS_END = WS_HHF + 2u * S * F;
static_assert(WS_CSP + 16 * 8 * 224 * 16 <= WS_URT, "ws layout");

constexpr int L_CS = 4160;
constexpr int L_FC = 0;
constexpr int L_TU = 66560;
constexpr int L_TQ = L_TU + 8192;
constexpr int L_WSTW = 16 * 288 + 16 * 144;
constexpr int L_WST = L_TQ + 8192;
constexpr int L_SCR = L_WST + 4 * L_WSTW;
constexpr int L_U = 114688;
constexpr int L_PART = L_U + 2 * 4608;
constexpr int L_MV = L_PART + 36864;
constexpr int L_RED = L_U;
constexpr int G_W = 28672;
constexpr int L_A = 4 * G_W;
constexpr int G_A = 16384;
constexpr int L_COR = L_A + 2 * G_A;
constexpr int LDS_BYTES = 163840;
static_assert(L_SCR + 4096 <= L_U && L_MV + 1024 <= LDS_BYTES, "lds layout");

constexpr int SPIN_CAP = 30000;
#define IF_(c) ((c) ? 0u : 0x80000000u)

#define BLD128(off, soff) __builtin_amdgcn_raw_buffer_load_b128(wsr, (off), (soff), 16)
#define BLD64(off, soff) __builtin_amdgcn_raw_buffer_load_b64(wsr, (off), (soff), 16)
#define BLD32(off, soff) __builtin_amdgcn_raw_buffer_load_b32(wsr, (off), (soff), 16)
#define BST128(v, off, soff) __builtin_amdgcn_raw_buffer_store_b128((v), wsr, (unsigned)(off) + (unsigned)(soff), 0, 16)
#define BST64(v, off, soff) __builtin_amdgcn_raw_buffer_store_b64((v), wsr, (off), (soff), 16)
#define BST32(v, off, soff) __builtin_amdgcn_raw_buffer_store_b32((v), wsr, (off), (soff), 16)
#define GDMA(voff, sbase, ldsa, imm)                                                                  \
  asm volatile("s_mov_b32 m0, %2\n\ts_nop 0\n\tglobal_load_lds_dwordx4 %0, %1 offset:" #imm " sc1"    \
               ::"v"(voff), "s"(sbase), "s"(ldsa) : "memory")

#define GDMAP(voff, sbase, ldsa)                                                                      \
  asm volatile("s_mov_b32 m0, %2\n\ts_nop 0\n\tglobal_load_lds_dwordx4 %0, %1"                        \
               ::"v"(voff), "s"(sbase), "s"(ldsa) : "memory")

#define GL_(dst, voff, sbase) asm volatile("global_load_dwordx4 %0, %1, %2" : "=v"(dst) : "v"(voff), "s"(sbase) : "memory")

DI constexpr int conv_n(int i) {
  return i < 8 ? 0 : i < 15 ? 1 : i < 21 ? 2 : i < 26 ? 3 : i < 30 ? 4 : i == 30 ? 5 : i == 31 ? 7 : i < 34 ? 5 : 6;
}
DI constexpr int conv_d(int i) {
  return i < 8 ? i : i < 15 ? i - 8 : i < 21 ? i - 15 : i < 26 ? i - 21 : i < 30 ? i - 26 : i < 32 ? 0 : i < 34 ? i - 31 : i - 34;
}
DI constexpr int wd_lds_off(int q, int w) { return (q / 7) * G_W + w * 7168 + (q % 7) * 1024; }
#define WEARLY 0
#define ORD 1
#define TCH 2
#define SGC 0
#define FRONT 1
#define WD_EARLY 0

__global__ __launch_bounds__(256, 1) __attribute__((amdgpu_waves_per_eu(1, 1))) void mega(
    const u16* __restrict__ q, const u16* __restrict__ k, const u16* __restrict__ v, const u16* __restrict__ shk,
    const u16* __restrict__ kf, const u16* __restrict__ ssmD, const u16* __restrict__ wg, const u16* __restrict__ wu,
    const u16* __restrict__ wd, const u16* __restrict__ cc, const u16* __restrict__ wmod, char* __restrict__ ws,
    u16* __restrict__ out) {
  extern __shared__ __attribute__((aligned(16))) char sm[];
  const int b = blockIdx.x, t = threadIdx.x;
  STAMP_DECL
  STAMP(0);
  const int w = __builtin_amdgcn_readfirstlane(t >> 6), lane = t & 63;
  const int xcd = b & 7, bi = b >> 3;
  const auto wsr = __builtin_amdgcn_make_buffer_rsrc(ws, 0, WS_END, 0x00020000);
  const int p = xcd * 32 + bi;
  const bool has_w = bi < 28;
  const int it = xcd * 28 + (has_w ? bi : 0);
  const int slab = it >> 3, kc = it & 7;
  const int rbk = (xcd & 1) * 8 + (bi & 7);
  const int cbk = (xcd >> 1) * 4 + (bi >> 3);
  const int sbk = xcd * 4 + (bi & 3);
  const int nbk = bi >> 2;
  const int g = lane >> 4, h = lane >> 5, qq = (lane & 15) >> 2, pp = lane & 3, l15 = lane & 15;

  const int st = rbk * 4 + w;
  const char* wsrc = (const char*)((w & 1) ? wu : wg) + (size_t)(32 * (w >> 1)) * (F * 2) + cbk * 224;
  unsigned wof[7];
#pragma unroll
  for (int j = 0; j < 7; ++j) {
    const int c_ = j * 64 + lane, rr_ = c_ / 14, cc_ = c_ - rr_ * 14;
    wof[j] = (unsigned)((rr_ - ((w & 1) ? 4 : 0)) & 31) * (F * 2) + cc_ * 16;
  }
  const char* asrc = ws + WS_X1S + (size_t)(8 * w) * (S * 4) + rbk * 512;
  const char* dsrc = ws + WS_WDF + ((size_t)(2 * nbk) * 112 + 28 * w) * 1024;
  const unsigned lbase = (unsigned)(uintptr_t)(__attribute__((address_space(3))) char*)sm;
  const unsigned ldw = lbase + w * 7168;
  const unsigned lda = lbase + L_A + w * 4096;
  const unsigned voff = lane * 16;
  const unsigned aoff = (lane >> 5) * (S * 4) + (lane & 31) * 16;

#define VW(g, j) { if ((j) >= 0 && (j) < 7) GDMAP(wof[(j) < 7 ? (j) : 0], wsrc + (size_t)(g) * (64 * F * 2), ldw + ((g) & 3) * G_W + (j) * 1024); }
#define VA(g, j) GDMA(aoff, asrc + (size_t)((g) * 32 + 2 * (j)) * (S * 4), lda + ((g) % 3) * G_A + (j) * 1024, 0);
#define VWALL(g) { VW(g, 0) VW(g, 1) VW(g, 2) VW(g, 3) VW(g, 4) VW(g, 5) VW(g, 6) }
#define VAALL(g) { VA(g, 0) VA(g, 1) VA(g, 2) VA(g, 3) }
#define WDM(q_) { if ((q_) >= 0 && (q_) < 20) GDMA(voff, dsrc + ((q_) & 1) * (112 * 1024) + ((q_) >> 1) * 1024, lbase + wd_lds_off((q_) < 0 ? 0 : (q_), w), 0); }

  u32 tag;
  u32x4 ka[2], kb[2], wl[6];
  u32 tpf;
  float gbv[16];
  const float* mv = (const float*)(sm + L_MV);
  if (t < 2) *(u32*)(sm + L_SCR + 2048 + t * 4) = 0u;
  {
    const int sb = xcd * 4 + (bi >> 3), db = bi & 7;
    const int rp = t >> 3, ci = t & 7;
    const int s0 = 64 * sb + 2 * rp, ch0 = 64 * db + 8 * ci;
    const u32 ep = BLD32(0, WS_EPOCH);
    u32x4 kr[5], vr[2], wr[4], qr[2];
#pragma unroll
    for (int i = 0; i < 5; ++i) {
      int s = s0 - 3 + i;
      kr[i] = *(const u32x4*)(k + (size_t)(s < 0 ? 0 : s) * D + ch0);
    }
#pragma unroll
    for (int i = 0; i < 2; ++i) vr[i] = *(const u32x4*)(v + (size_t)(s0 + i) * D + ch0);
#pragma unroll
    for (int i = 0; i < 4; ++i) wr[i] = *(const u32x4*)(shk + (size_t)ch0 * 4 + i * 8);
#pragma unroll
    for (int i = 0; i < 2; ++i) qr[i] = *(const u32x4*)(q + (size_t)(s0 + i) * D + ch0);
    u32 cpf;
    {
      u64 c0;
      int r0_;
      asm volatile("s_getpc_b64 %0\n\ts_mov_b32 %1, _Z5k_endPc@rel32@lo+4" : "=s"(c0), "=s"(r0_));
      const unsigned off = (unsigned)(bi * 20 + (t & 31)) * 64;
      const char* cp_ = (const char*)(c0 & ~63ull) + (off + 64 <= (unsigned)r0_ ? off : 0u);
      asm volatile("global_load_dword %0, %1, off" : "=v"(cpf) : "v"(cp_) : "memory");
    }
    SCHED_FENCE();
    u32* Tu = (u32*)(sm + L_TU);
    u32* Tq = (u32*)(sm + L_TQ);
    const int x = 31 - rp;
    const char* kfp = (const char*)(kf + (size_t)(2 * p) * S);
    unsigned kfa = (t > 0 ? t - 1 : 0) * 16, kfb = t * 16;
    asm volatile("" : "+v"(kfa), "+v"(kfb));
    const int r0 = 8 * b + 2 * w;
    const int mi = b < 192 ? b : 0;
    const int mslab = mi % 48, mq = mi / 48;
    unsigned mo = ((unsigned)(128 * mq + 4 * rp) * (6 * D) + 64 * mslab + 8 * ci) * 2, xo = ((unsigned)r0 * D + lane * 8) * 2;
    asm volatile("" : "+v"(mo), "+v"(xo));
    asm volatile("s_waitcnt vmcnt(0)" : "+v"(cpf), "+v"(wr[3]), "+v"(vr[1]), "+v"(kr[4]), "+v"(qr[1])::"memory");
    SCHED_FENCE();
    u32x4 xr[2], wv[4];
    u32x2 cv;
#define LD_KF()                                                         \
  _Pragma("unroll") for (int ch = 0; ch < 2; ++ch) {                    \
    ka[ch] = *(const u32x4*)(kfp + ch * (S * 2) + kfa);                 \
    kb[ch] = *(const u32x4*)(kfp + ch * (S * 2) + kfb);                 \
  }
#define LD_MX()                                                                                              \
  _Pragma("unroll") for (int i = 0; i < 4; ++i) wv[i] = *(const u32x4*)((const char*)wmod + i * (6 * D * 2) + mo); \
  cv = *(const u32x2*)(cc + 128 * mq + 4 * rp);                                                              \
  _Pragma("unroll") for (int i = 0; i < 2; ++i) xr[i] = *(const u32x4*)((const char*)q + i * (D * 2) + xo);
    LD_KF()
    SCHED_FENCE();
    tag = __builtin_amdgcn_readfirstlane(ep) + 1u;
#pragma unroll
    for (int i = 0; i < 3; ++i) {
      const bool neg = (s0 - 3 + i) < 0;
#pragma unroll
      for (int j = 0; j < 4; ++j) kr[i][j] = neg ? 0u : kr[i][j];
    }
#pragma unroll
    for (int j = 0; j < 8; ++j) {
      const u32 w01 = wr[j >> 1][(j & 1) * 2], w23 = wr[j >> 1][(j & 1) * 2 + 1];
      const float w0 = lo2f(w01), w1 = hi2f(w01), w2 = lo2f(w23), w3 = hi2f(w23);
      float kk[5];
#pragma unroll
      for (int i = 0; i < 5; ++i) kk[i] = (j & 1) ? hi2f(kr[i][j >> 1]) : lo2f(kr[i][j >> 1]);
      f32x2 a = rb2((f32x2){kk[0], kk[1]} * w3);
      a = rb2(a + rb2((f32x2){kk[1], kk[2]} * w2));
      a = rb2(a + rb2((f32x2){kk[2], kk[3]} * w1));
      a = rb2(a + rb2((f32x2){kk[3], kk[4]} * w0));
      const float v0 = (j & 1) ? hi2f(vr[0][j >> 1]) : lo2f(vr[0][j >> 1]);
      const float v1 = (j & 1) ? hi2f(vr[1][j >> 1]) : lo2f(vr[1][j >> 1]);
      const u32 u = pk2(a[1] * v1, a[0] * v0);
      Tu[(8 * ci + j) * 32 + ((((x >> 2) ^ ci) << 2) | (x & 3))] = u;
    }
#pragma unroll
    for (int j = 0; j < 4; ++j) {
      const int xs = 2 * rp;
      u32x2 val = {qr[0][j], qr[1][j]};
      *(u32x2*)&Tq[(4 * ci + j) * 64 + ((((xs >> 2) ^ (2 * ci)) << 2) | (xs & 3))] = val;
    }
    STAMP(1);
    RAWBAR();
    {
      const int tbase = S - 64 * (sb + 1);
#pragma unroll
      for (int i = 0; i < 2; ++i) {
        const int idx = t + 256 * i;
        const int ch = idx >> 3, c8 = idx & 7;
        u32x4 val = *(const u32x4*)&Tu[ch * 32 + ((c8 ^ (ch >> 3)) << 2)];
        BST128(val, ((64 * db + ch) * S + tbase + c8 * 8) * 2, WS_URT);
        const int pr = idx >> 4, c16 = idx & 15;
        u32x4 qv = *(const u32x4*)&Tq[pr * 64 + ((c16 ^ (2 * (pr >> 2))) << 2)];
        BST128(qv, ((32 * db + pr) * S + 64 * sb + c16 * 4) * 4, WS_QT2);
      }
    }
    SCHED_FENCE();
    LD_MX()
    SCHED_FENCE();
    asm volatile("s_waitcnt vmcnt(7)" ::: "memory");
    RAWBAR();
    BST32(tag, (b * 64) | IF_(t == 0), WS_FLA);
    SCHED_FENCE();
    SCHED_FENCE();
    STAMP(2);
#pragma unroll
    for (int ch = 0; ch < 2; ++ch) {
      u32 fv[8] = {ka[ch][0], ka[ch][1], ka[ch][2], ka[ch][3], kb[ch][0], kb[ch][1], kb[ch][2], kb[ch][3]};
#pragma unroll
      for (int i = 0; i < 4; ++i) fv[i] = t > 0 ? fv[i] : 0u;
      u32 od[7];
#pragma unroll
      for (int i = 0; i < 7; ++i) od[i] = __builtin_amdgcn_alignbit(fv[i + 1], fv[i], 16);
      char* base = sm + L_FC + ch * (8 * L_CS) + (t + 1) * 16;
#pragma unroll
      for (int r = 0; r < 8; ++r) {
        const int sg = r + 1;
        u32x4 val;
        if (sg & 1) {
          const int k0 = (sg - 1) / 2;
          val = (u32x4){od[k0], od[k0 + 1], od[k0 + 2], od[k0 + 3]};
        } else {
          const int k0 = sg / 2;
          val = (u32x4){fv[k0], fv[k0 + 1], fv[k0 + 2], fv[k0 + 3]};
        }
        *(u32x4*)(base + r * L_CS) = val;
      }
    }
    {
      const u32x4 z = {0, 0, 0, 0};
      if (t < 16) *(u32x4*)(sm + L_FC + t * L_CS) = z;
      if (t >= 64 && t < 128) *(u32x4*)(sm + L_U + ((t >> 5) & 1) * 4608 + 4096 + (t & 31) * 16) = z;
    }
    SCHED_FENCE();

    const u32 dpair = ((const u32*)ssmD)[p];
    SCHED_FENCE();
    {
      float acc[8];
#pragma unroll
      for (int j = 0; j < 8; ++j) acc[j] = 0.f;
#pragma unroll
      for (int i = 0; i < 4; ++i) {
        const float xx = (i & 1) ? hi2f(cv[i >> 1]) : lo2f(cv[i >> 1]);
        const float sl = rb(xx * __builtin_amdgcn_rcpf(1.f + __expf(-xx)));
#pragma unroll
        for (int j = 0; j < 4; ++j) {
          acc[2 * j] += sl * lo2f(wv[i][j]);
          acc[2 * j + 1] += sl * hi2f(wv[i][j]);
        }
      }
#pragma unroll
      for (int j = 0; j < 8; ++j) acc[j] = red_8_16_32(acc[j]);
      if (lane < 8) {
        float* red = (float*)(sm + L_SCR) + w * 64 + lane * 8;
        *(f32x4*)red = (f32x4){acc[0], acc[1], acc[2], acc[3]};
        *(f32x4*)(red + 4) = (f32x4){acc[4], acc[5], acc[6], acc[7]};
      }
      __builtin_amdgcn_fence(__ATOMIC_RELEASE, "workgroup");
      u32 old = 0;
      if (lane == 0) old = __hip_atomic_fetch_add((u32*)(sm + L_SCR + 2052), 1u, __ATOMIC_RELAXED, __HIP_MEMORY_SCOPE_WORKGROUP);
      old = __builtin_amdgcn_readfirstlane(old);
      __builtin_amdgcn_fence(__ATOMIC_ACQUIRE, "workgroup");
      const float* red = (const float*)(sm + L_SCR);
      const float r = (red[lane] + red[64 + lane]) + (red[128 + lane] + red[192 + lane]);
      u32x2 gr = {__float_as_uint(r), tag};
      BST64(gr, ((mq * 3072 + 64 * mslab + lane) * 8) | IF_(old == 3 && b < 192), WS_MODG);
    }
    {
      float mean[2], var[2];
#pragma unroll
      for (int i = 0; i < 2; ++i) {
        float a = 0.f;
#pragma unroll
        for (int j = 0; j < 4; ++j) a = dot2(xr[i][j], 0x3f803f80u, a);
        mean[i] = wave_sum(a) * (1.f / D);
      }
#pragma unroll
      for (int i = 0; i < 2; ++i) {
        f32x2 a = {0.f, 0.f};
#pragma unroll
        for (int j = 0; j < 4; ++j) {
          const f32x2 d = up2(xr[i][j]) - mean[i];
          a += d * d;
        }
        var[i] = wave_sum(a[0] + a[1]) * (1.f / D);
      }
      const float m_ = lane == 0 ? mean[0] : mean[1], v_ = lane == 0 ? var[0] : var[1];
      const float sd = __builtin_amdgcn_sqrtf(rb(rb(v_) + 1e-5f));
      u32x2 gr = {pk2(m_, sd), tag};
      BST64(gr, ((r0 + lane) * 8) | IF_(lane < 2), WS_ST1G);
    }
    STAMP(3);
#pragma nounroll
    for (int spins = 0; spins < SPIN_CAP; ++spins) {
      const u32 f = BLD32(((((lane & 3) * 8 + xcd) * 8) + ((lane & 31) >> 2)) * 64, WS_FLA);
      if (__all(f == tag)) break;
      __builtin_amdgcn_s_sleep(1);
    }
    __builtin_amdgcn_fence(__ATOMIC_ACQUIRE, "wavefront");
    u32x4 uu[2], qp[2];
#pragma unroll
    for (int ch = 0; ch < 2; ++ch) uu[ch] = BLD128(((2 * p + ch) * S + t * 8) * 2, WS_URT);
#pragma unroll
    for (int i = 0; i < 2; ++i) qp[i] = BLD128((p * S + 1024 * i + 4 * t) * 4, WS_QT2);
    SCHED_FENCE();
    STAMP(4);
    SCHED_FENCE();
    const int cch = w >> 1, c0 = 4 * (w & 1);
    const char* fb = sm + L_FC + cch * (8 * L_CS) + (l15 & 7) * L_CS + 2 * ((l15 & 8) + 8 * g) + 64 * c0;
    const char* gb = sm + L_U + cch * 4608 + 2 * (2032 - 256 * 7 - 16 * l15 + 8 * g) + 64 * c0;
    u32x4 df;
    {
      const u32 dv = c0 == 0 ? (cch ? (dpair >> 16) : (dpair & 0xffffu)) : 0u;
      const int kap = 15 - l15 - 8 * g;
#pragma unroll
      for (int j = 0; j < 4; ++j) df[j] = (kap == 2 * j) ? dv : (kap == 2 * j + 1) ? (dv << 16) : 0u;
    }
    asm volatile("" : "+v"(df));
#pragma unroll
    for (int ch = 0; ch < 2; ++ch) *(u32x4*)(sm + L_U + ch * 4608 + t * 16) = uu[ch];
    STAMP(5);
    RAWBAR();
    STAMP(6);
    const int me = t < 64 ? 4 * D + 64 * kc + t
                 : t < 128 ? 3 * D + 64 * kc + (t - 64)
                 : t < 192 ? 5 * D + 64 * nbk + (t - 128)
                           : ((((t - 192) >> 1) & 3) == 3 ? 4 : (((t - 192) >> 1) & 3)) * D + 2 * p + (t & 1);
    u32x4 sg[4];
    u32x2 mg[4];
    f32x4 acc[8];
    {
      bf16x8 Fr[2][8], Gr[2][8];
#pragma unroll
      for (int n = 0; n < 8; ++n) Gr[0][n] = *(const bf16x8*)(gb + 512 * (7 - n));
#pragma unroll
      for (int n = 0; n < 8; ++n) Fr[0][n] = *(const bf16x8*)(fb + 512 * n);
      SCHED_FENCE();
#pragma unroll
      for (int m = 0; m < 8; ++m)
        acc[m] = __builtin_amdgcn_mfma_f32_16x16x32_bf16(__builtin_bit_cast(bf16x8, df), Gr[0][m],
                                                         (f32x4){0.f, 0.f, 0.f, 0.f}, 0, 0, 0);
      SCHED_FENCE();
#pragma unroll
      for (int c = 0; c < 4; ++c) {
        const int cur = c & 1, nxt = cur ^ 1;
#pragma unroll
        for (int i = 0; i < 36; ++i) {
          const int n = conv_n(i), dl = conv_d(i);
          acc[n + dl] = __builtin_amdgcn_mfma_f32_16x16x32_bf16(Fr[cur][n], Gr[cur][dl], acc[n + dl], 0, 0, 0);
          if (c < 3 && (i & 1) && i < 32) {
            const int j = i >> 1;
            if (j == 0) Fr[nxt][0] = *(const bf16x8*)(fb + 64 * (c + 1));
            else if (j <= 8) Gr[nxt][j - 1] = *(const bf16x8*)(gb + 512 * (8 - j) + 64 * (c + 1));
            else Fr[nxt][j - 8] = *(const bf16x8*)(fb + 512 * (j - 8) + 64 * (c + 1));
          }
          if (i & 1) SCHED_FENCE();
        }
        if (c == TCH) {
          asm volatile("" : "+v"(sg[0]), "+v"(sg[1]), "+v"(sg[2]), "+v"(sg[3]), "+v"(mg[0]), "+v"(mg[1]), "+v"(mg[2]), "+v"(mg[3]), "+v"(qp[0]), "+v"(qp[1]));
          const char* tp = t < 64 ? (const char*)(wg + (size_t)(64 * kc + t) * F + 64 * slab)
                         : t < 128 ? (const char*)(wu + (size_t)(64 * kc + t - 64) * F + 64 * slab)
                                   : (const char*)(wd + (size_t)(64 * slab + (t & 63)) * D + 64 * kc);
          asm volatile("global_load_dword %0, %1, off" : "=v"(tpf) : "v"(tp) : "memory");
          SCHED_FENCE();
        }
        if (c == SGC) {
#pragma unroll
          for (int i = 0; i < 2; ++i) {
            sg[2 * i] = BLD128((1024 * i + 4 * t) * 8, WS_ST1G);
            sg[2 * i + 1] = BLD128((1024 * i + 4 * t) * 8 + 16, WS_ST1G);
          }
#pragma unroll
          for (int i = 0; i < 4; ++i) mg[i] = BLD64(me * 8, WS_MODG + i * 3072 * 8);
          SCHED_FENCE();
        }
      }
    }
    {
      float* part = (float*)(sm + L_PART) + w * (8 * 288) + g * 72 + l15 * 4;
#pragma unroll
      for (int m = 0; m < 8; ++m) *(f32x4*)(part + m * 288) = acc[m];
    }
    STAMP(7);
    {
      bool ok = true;
#pragma unroll
      for (int i = 0; i < 4; ++i) ok = ok && mg[i][1] == tag && sg[i][1] == tag && sg[i][3] == tag;
      if (__builtin_expect(!__all(ok), 0)) {
#pragma nounroll
        for (int spins = 0; spins < SPIN_CAP; ++spins) {
#pragma unroll
          for (int i = 0; i < 4; ++i) mg[i] = BLD64(me * 8, WS_MODG + i * 3072 * 8);
#pragma unroll
          for (int i = 0; i < 2; ++i) {
            sg[2 * i] = BLD128((1024 * i + 4 * t) * 8, WS_ST1G);
            sg[2 * i + 1] = BLD128((1024 * i + 4 * t) * 8 + 16, WS_ST1G);
          }
          asm volatile("" : "+v"(sg[0]), "+v"(sg[1]), "+v"(sg[2]), "+v"(sg[3]), "+v"(mg[0]), "+v"(mg[1]), "+v"(mg[2]), "+v"(mg[3]));
          bool ok2 = true;
#pragma unroll
          for (int i = 0; i < 4; ++i) ok2 = ok2 && mg[i][1] == tag && sg[i][1] == tag && sg[i][3] == tag;
          if (__all(ok2)) break;
          __builtin_amdgcn_s_sleep(2);
        }
      }
    }
    {
      float val = rb((__uint_as_float(mg[0][0]) + __uint_as_float(mg[1][0])) + (__uint_as_float(mg[2][0]) + __uint_as_float(mg[3][0])));
      if (t < 64 || (t >= 192 && (t & 2))) val = rb(1.f + val);
      ((float*)(sm + L_MV))[t] = val;
    }
    f32x2 qn[8];
#pragma unroll
    for (int i = 0; i < 8; ++i) {
      const u32 ms = sg[i >> 1][(i & 1) * 2];
      const float mb = lo2f(ms), rs = __builtin_amdgcn_rcpf(hi2f(ms));
      qn[i] = rb2(rb2(up2(qp[i >> 2][i & 3]) - mb) * rs);
      asm volatile("" : "+v"(qn[i]));
    }
    RAWBAR();
    SCHED_FENCE();
    const f32x2 sa = {mv[192], mv[193]}, oc = {mv[194], mv[195]}, ga = {mv[196], mv[197]}, ob = {mv[198], mv[199]};
    {
      const float* part = (const float*)(sm + L_PART) + (t >> 6) * 288 + (t & 3) * 72 + ((t >> 2) & 15) * 4;
#pragma unroll
      for (int i = 0; i < 2; ++i) {
        const f32x4 y0 = *(const f32x4*)(part + i * 4 * 288) + *(const f32x4*)(part + 2304 + i * 4 * 288);
        const f32x4 y1 = *(const f32x4*)(part + 2 * 2304 + i * 4 * 288) + *(const f32x4*)(part + 3 * 2304 + i * 4 * 288);
        u32x4 o, os;
#pragma unroll
        for (int e = 0; e < 4; ++e) {
          const f32x2 hh_ = rb2(rb2(qn[4 * i + e] * oc) + sa);
          const f32x2 z = rb2((f32x2){y0[e], y1[e]});
          const f32x2 a = rb2(rb2(hh_ * z) * ga);
          const f32x2 x1 = rb2(up2(qp[i][e]) + a);
          o[e] = __builtin_amdgcn_perm(__float_as_uint(x1[1]), __float_as_uint(x1[0]), 0x07060302u);
          os[e] = pk2v(x1 * ob);
        }
        BST128(os, (p * S + 1024 * i + 4 * t) * 4, WS_X1S);
        BST128(o, (p * S + 1024 * i + 4 * t) * 4, WS_X1P);
      }
    }
  }
  SCHED_FENCE();
  STAMP(8);

  asm volatile("" :: "v"(tpf));
  DRAINBAR();
  BST32(tag, (b * 64) | IF_(t == 0), WS_FL1);
  SCHED_FENCE();
    {
      const unsigned wo1 = ((unsigned)(16 * w + (lane >> 3)) * F + 8 * (lane & 7)) * 2;
      const unsigned wo2 = ((unsigned)(16 * w + (lane >> 3)) * D + 8 * (lane & 7)) * 2;
      const char* wgb = (const char*)(wg + (size_t)(64 * kc) * F + 64 * slab);
      const char* wub = (const char*)(wu + (size_t)(64 * kc) * F + 64 * slab);
      const char* wdb = (const char*)(wd + (size_t)(64 * slab) * D + 64 * kc);
      GL_(wl[0], wo1, wgb);
      GL_(wl[1], wo1, wgb + 8 * F * 2);
      GL_(wl[2], wo1, wub);
      GL_(wl[3], wo1, wub + 8 * F * 2);
      GL_(wl[4], wo2, wdb);
      GL_(wl[5], wo2, wdb + 8 * D * 2);
    }
  SCHED_FENCE();
  VWALL(0)
  VWALL(1)
  SCHED_FENCE();
  u32x4 wr2[36];
  {
    const int fn = w >> 1;
#pragma unroll
    for (int i = 0; i < 4; ++i) {
      const f32x4 gq = *(const f32x4*)(mv + 128 + 32 * fn + 8 * h + (i >> 1) * 16 + (i & 1) * 4);
#pragma unroll
      for (int e = 0; e < 4; ++e) gbv[4 * i + e] = gq[e];
    }
    char* wst = sm + 3 * G_W + w * 7168;
    asm volatile("s_waitcnt vmcnt(%6)" : "+v"(wl[0]), "+v"(wl[1]), "+v"(wl[2]), "+v"(wl[3]), "+v"(wl[4]), "+v"(wl[5]) : "n"(7 * (2 - WEARLY)) : "memory");
    {
      const int row = lane >> 3, c16 = lane & 7;
      char* d1 = wst + row * 288 + c16 * 16;
      *(u32x4*)(d1) = wl[0];
      *(u32x4*)(d1 + 8 * 288) = wl[1];
      *(u32x4*)(d1 + 128) = wl[2];
      *(u32x4*)(d1 + 8 * 288 + 128) = wl[3];
      char* d2 = wst + 16 * 288 + row * 144 + c16 * 16;
      *(u32x4*)(d2) = wl[4];
      *(u32x4*)(d2 + 8 * 144) = wl[5];
    }
    __builtin_amdgcn_fence(__ATOMIC_RELEASE, "wavefront");
    __builtin_amdgcn_wave_barrier();
    __builtin_amdgcn_fence(__ATOMIC_ACQUIRE, "wavefront");
#pragma unroll
    for (int j = 0; j < 2; ++j) {
      const char* a0 = wst + 16 * 288 + (4 * h + qq) * 144 + j * 64 + (g & 1) * 32 + pp * 8;
      s16x4 lo = __builtin_amdgcn_ds_read_tr16_b64_v4i16((lds_s4p)(a0));
      s16x4 hi = __builtin_amdgcn_ds_read_tr16_b64_v4i16((lds_s4p)(a0 + 8 * 144));
      u32x2 l2 = __builtin_bit_cast(u32x2, lo), h2 = __builtin_bit_cast(u32x2, hi);
      u32x4 o = {l2[0], l2[1], h2[0], h2[1]};
      BST128(o, (lane * 16) | IF_(has_w), WS_WDF + ((2 * kc + j) * 112 + 4 * slab + w) * 1024);
    }
    const f32x4 sc0 = *(const f32x4*)(mv + 16 * w + 8 * h), sc1 = *(const f32x4*)(mv + 16 * w + 8 * h + 4);
    const f32x4 sb0 = *(const f32x4*)(mv + 64 + 16 * w + 8 * h), sb1 = *(const f32x4*)(mv + 64 + 16 * w + 8 * h + 4);
    const u32 scv[4] = {pk2(sc0[0], sc0[1]), pk2(sc0[2], sc0[3]), pk2(sc1[0], sc1[1]), pk2(sc1[2], sc1[3])};
    const u32 sbv[4] = {pk2(sb0[0], sb0[1]), pk2(sb0[2], sb0[3]), pk2(sb1[0], sb1[1]), pk2(sb1[2], sb1[3])};
#pragma unroll
    for (int j = 0; j < 4; ++j) {
      const char* a0 = wst + (8 * h + qq) * 288 + (g & 1) * 128 + j * 32 + pp * 8;
      s16x4 lo = __builtin_amdgcn_ds_read_tr16_b64_v4i16((lds_s4p)(a0));
      s16x4 hi = __builtin_amdgcn_ds_read_tr16_b64_v4i16((lds_s4p)(a0 + 4 * 288));
      u32x2 l2 = __builtin_bit_cast(u32x2, lo), h2 = __builtin_bit_cast(u32x2, hi);
      const u32 wv4[4] = {l2[0], l2[1], h2[0], h2[1]};
      float cs = 0.f, bs = 0.f;
#pragma unroll
      for (int e = 0; e < 4; ++e) {
        cs = dot2(wv4[e], scv[e], cs);
        bs = dot2(wv4[e], sbv[e], bs);
      }
      *(f32x2*)(sm + L_RED + ((w * 4 + j) * 64 + lane) * 8) = (f32x2){cs, bs};
    }
    RAWBAR();
    const f32x2* red = (const f32x2*)(sm + L_RED);
    f32x2 rr = (red[(0 * 4 + w) * 64 + lane] + red[(1 * 4 + w) * 64 + lane]) +
               (red[(2 * 4 + w) * 64 + lane] + red[(3 * 4 + w) * 64 + lane]);
    auto x0 = __builtin_amdgcn_permlane32_swap(__float_as_uint(rr[0]), __float_as_uint(rr[0]), false, false);
    auto x1 = __builtin_amdgcn_permlane32_swap(__float_as_uint(rr[1]), __float_as_uint(rr[1]), false, false);
    u32x4 o = {__float_as_uint(__uint_as_float(x0[0]) + __uint_as_float(x0[1])), tag,
               __float_as_uint(__uint_as_float(x1[0]) + __uint_as_float(x1[1])), tag};
    const int mtg = 4 * slab + w;
    BST128(o, (lane * 16) | IF_(has_w && lane < 32), WS_CSP + (((mtg / 7) * 8 + kc) * 224 + (mtg % 7) * 32) * 16);
  }
  VWALL(2)
  STAMP(9);
#pragma nounroll
  for (int spins = 0; spins < SPIN_CAP; ++spins) {
    const u32 f = BLD32(t * 64, WS_FL1);
    if (__all(f == tag)) break;
    __builtin_amdgcn_s_sleep(1);
  }
  DRAINBAR();
  STAMP(10);
  BST32(tag, t == 0 ? WS_FLW + b * 64 : (b == 0 && t == 1) ? WS_EPOCH : 0x80000000u, 0);
  VAALL(0)
  u32x4 sx[2];
  {
    const char* sbase = ws + WS_X1P + (size_t)(rbk * 128 + cbk * 8) * 4;
    const unsigned so = (unsigned)(t >> 1) * (S * 4) + (t & 1) * 16;
    asm volatile("global_load_dwordx4 %0, %1, %2 sc1" : "=v"(sx[0]) : "v"(so), "s"(sbase) : "memory");
    asm volatile("global_load_dwordx4 %0, %1, %2 sc1" : "=v"(sx[1]) : "v"(so), "s"(sbase + (size_t)128 * (S * 4)) : "memory");
  }
  VAALL(1)

  f32x16 acc[7];
#pragma unroll
  for (int m = 0; m < 7; ++m)
#pragma unroll
    for (int r = 0; r < 16; ++r) acc[m][r] = 0.f;
  const char* lsrc = sm + lane * 16;
  const char* lsa = sm + L_A + (4 * h * 128 + 32 * w + (lane & 31)) * 4;
  bf16x8 wf[2][7];
  u32x4 af[2];

#define WAITG(n)                                                              \
  SCHED_FENCE();                                                              \
  asm volatile("s_waitcnt vmcnt(" #n ") lgkmcnt(0)" ::: "memory");            \
  __builtin_amdgcn_s_barrier();                                               \
  asm volatile("" ::: "memory");                                              \
  SCHED_FENCE();
#define KOFF(i) ((((i) >> 2) & 1) * G_W + (((i) >> 1) & 1) * 14336)
  unsigned lw[2][2][2];
#pragma unroll
  for (int sp = 0; sp < 2; ++sp)
#pragma unroll
    for (int kp = 0; kp < 2; ++kp)
#pragma unroll
      for (int hi_ = 0; hi_ < 2; ++hi_) {
        lw[sp][kp][hi_] = lbase + sp * 2 * G_W + (g & 1) * 7168 + ((16 * kp + 8 * h + 4 * hi_ + qq + (g & 1) * 4) & 31) * 224 + pp * 8;
        asm volatile("" : "+v"(lw[sp][kp][hi_]));
      }
#define TRR(i, m, hi_) __builtin_amdgcn_ds_read_tr16_b64_v4i16((lds_s4p)(uintptr_t)(lw[((i) >> 3) & 1][(i) & 1][hi_] + KOFF(i) + (m) * 32))
#define TRF(i, m) __builtin_shufflevector(TRR(i, m, 0), TRR(i, m, 1), 0, 1, 2, 3, 4, 5, 6, 7)
#define AOFF(i) ((((i) >> 2) % 3) * G_A + ((i) & 3) * 4096)
#define RD(i, m) wf[(i) & 1][m] = TRF(i, m);
#define RA(dst, i)                                                                   \
  {                                                                                  \
    dst[0] = *(const u32*)(lsa + AOFF(i));                                           \
    dst[1] = *(const u32*)(lsa + AOFF(i) + 512);                                     \
    dst[2] = *(const u32*)(lsa + AOFF(i) + 1024);                                    \
    dst[3] = *(const u32*)(lsa + AOFF(i) + 1536);                                    \
  }
#define MM(i, m) acc[m] = __builtin_amdgcn_mfma_f32_32x32x16_bf16(wf[(i) & 1][m], __builtin_bit_cast(bf16x8, af[(i) & 1]), acc[m], 0, 0, 0);
#define VX(ga, gw, q0, j)                                                                        \
  {                                                                                              \
    if ((ga) >= 0 && (j) < 4) VA((ga) < 0 ? 0 : (ga), (j) & 3)                                   \
    if ((gw) >= 0 && (j) >= 4 && (j) < 11) VW((gw) < 0 ? 0 : (gw), (j) - 4)                      \
    if ((q0) >= 0 && (j) >= 4 && (j) < 11) WDM((q0) + (j) - 4)                                   \
  }
#define STEPX(i, ga, gw, q0, j0, X)                                                    \
  {                                                                                    \
    MM(i, 0) RA(af[((i) + 1) & 1], (i) + 1) RD((i) + 1, 0) SCHED_FENCE();              \
    MM(i, 1) RD((i) + 1, 1) RD((i) + 1, 2) SCHED_FENCE();                              \
    MM(i, 2) RD((i) + 1, 3) RD((i) + 1, 4) SCHED_FENCE();                              \
    MM(i, 3) RD((i) + 1, 5) X SCHED_FENCE();                                           \
    MM(i, 4) RD((i) + 1, 6) VX(ga, gw, q0, j0) SCHED_FENCE();                          \
    MM(i, 5) VX(ga, gw, q0, (j0) + 1) SCHED_FENCE();                                   \
    MM(i, 6) VX(ga, gw, q0, (j0) + 2) SCHED_FENCE();                                   \
  }
#define STEP(i, ga, gw, q0, j0) STEPX(i, ga, gw, q0, j0, )
#define STEP4X(i, ga, gw, q0, X0, X1, X2, X3) STEPX(i, ga, gw, q0, 0, X0) STEPX((i) + 1, ga, gw, q0, 3, X1) STEPX((i) + 2, ga, gw, q0, 6, X2) STEPX((i) + 3, ga, gw, q0, 9, X3)
#define STEP4(i, ga, gw, q0) STEP4X(i, ga, gw, q0, , , , )
  SCHED_FENCE();
  asm volatile("s_waitcnt vmcnt(4)" : "+v"(sx[0]), "+v"(sx[1])::"memory");
  {
    float sv[8];
#pragma unroll
    for (int e = 0; e < 4; ++e) {
      sv[e] = red_2_64(dot2(sx[1][e], 0x3f803f80u, dot2(sx[0][e], 0x3f803f80u, 0.f)));
      sv[4 + e] = red_2_64(dot2(sx[1][e], sx[1][e], dot2(sx[0][e], sx[0][e], 0.f)));
    }
    if (lane < 2) {
      float* ps = (float*)(sm + L_A + 2 * G_A) + (w * 2 + lane) * 8;
      *(f32x4*)ps = (f32x4){sv[0], sv[1], sv[2], sv[3]};
      *(f32x4*)(ps + 4) = (f32x4){sv[4], sv[5], sv[6], sv[7]};
    }
  }
  WAITG(4)
  STAMP(11);
  RA(af[0], 0)
  RD(0, 0) RD(0, 1) RD(0, 2) RD(0, 3) RD(0, 4) RD(0, 5) RD(0, 6)
  if (w == 0) {
    const float* ps = (const float*)(sm + L_A + 2 * G_A) + ((lane >> 2) & 1) * 8 + (lane & 3);
    const float s1 = (ps[0] + ps[16]) + (ps[32] + ps[48]);
    const float s2 = (ps[4] + ps[20]) + (ps[36] + ps[52]);
    const float mean = s1 * (1.f / D);
    const float var = fmaxf(s2 * (1.f / D) - mean * mean, 0.f);
    const float sdv = __builtin_amdgcn_sqrtf(rb(rb(var) + 1e-5f));
    u32x2 gr = {pk2(mean, sdv), tag};
    if (lane < 8) BST64(gr, (rbk * 128 + cbk * 8 + lane) * 8, WS_ST2G);
    asm volatile("s_waitcnt lgkmcnt(0)" ::: "memory");
  }
  SCHED_FENCE();
  VX(2, 3, -1, 0) VX(2, 3, -1, 1)
  STEP(0, 2, 3, -1, 2) STEP(1, 2, 3, -1, 5) STEP(2, 2, 3, -1, 8)
  WAITG(11)
  STEP4(3, 3, 4, -1)
  WAITG(18)
  u32 fw;
  u32x2 stg;
  u32x4 cp[8];
  const char* stp = ws + WS_ST2G + (size_t)st * 256;
  const unsigned sto = (lane & 31) * 8;
  const char* cbase = ws + WS_CSP + (size_t)cbk * (8 * 224 * 16);
  const unsigned co = (t < 224 ? t : 0) * 16;
  unsigned fwo;
  {
    const int it_ = (7 * w + (lane < 7 ? lane : 0)) * 8 + nbk;
    fwo = WS_FLW + ((it_ % 28) * 8 + it_ / 28) * 64;
  }
#define CPL(i) asm volatile("global_load_dwordx4 %0, %1, %2 sc1" : "=v"(cp[i]) : "v"(co), "s"(cbase + (i) * 3584) : "memory");
#define STGL() asm volatile("global_load_dwordx2 %0, %1, %2 sc1" : "=v"(stg) : "v"(sto), "s"(stp) : "memory");
#define FWL() asm volatile("buffer_load_dword %0, %1, %2, 0 offen sc1" : "=v"(fw) : "v"(fwo), "s"(wsr) : "memory");
  STEP4X(7, 4, 5, -1, CPL(0), CPL(1), CPL(2), CPL(3))
  WAITG(22)
  STEP4X(11, 5, 6, -1, CPL(4), CPL(5), CPL(6), CPL(7))
  WAITG(22)
  STEP4X(15, 6, 7, -1, FWL(), STGL(), , )
  asm volatile("s_waitcnt vmcnt(12)" : "+v"(fw), "+v"(cp[0]), "+v"(cp[1]), "+v"(cp[2]), "+v"(cp[3]), "+v"(cp[4]), "+v"(cp[5]), "+v"(cp[6]), "+v"(cp[7])::"memory");
  asm volatile("" : "+a"(acc[0]), "+a"(acc[1]), "+a"(acc[2]), "+a"(acc[3]), "+a"(acc[4]), "+a"(acc[5]), "+a"(acc[6]));
  {
    bool ok = fw == tag;
#pragma unroll
    for (int i = 0; i < 8; ++i) ok = ok && cp[i][1] == tag && cp[i][3] == tag;
    if (__builtin_expect(!__all(ok), 0)) {
#pragma nounroll
      for (int spins = 0; spins < SPIN_CAP; ++spins) {
        __builtin_amdgcn_s_sleep(1);
        fw = BLD32(fwo, 0);
#pragma unroll
        for (int i = 0; i < 8; ++i) cp[i] = BLD128(co, WS_CSP + (cbk * 8 + i) * 3584);
        bool ok2 = fw == tag;
#pragma unroll
        for (int i = 0; i < 8; ++i) ok2 = ok2 && cp[i][1] == tag && cp[i][3] == tag;
        if (__all(ok2)) break;
      }
    }
  }
  WAITG(12)
  float cs = 0.f, bs = 0.f;
  u32x4 corr;
#define CSUM(i) { cs += __uint_as_float(cp[i][0]); bs += __uint_as_float(cp[i][2]); }
  STEP4X(19, 7, -1, 0, CSUM(0) CSUM(1) CSUM(2), CSUM(3) CSUM(4) CSUM(5), CSUM(6) CSUM(7),
         {
           const float c0 = rb(cs); const float c1 = rb(cs - c0); const float c2 = rb(cs - c0 - c1);
           const float b0 = rb(bs); const float b1 = rb(bs - b0); const float b2 = rb(bs - b0 - b1);
           corr = ((u32x4){pk2(c0, c1), pk2(c2, b0), pk2(b1, b2), 0u});
         })
  WAITG(11)
  STEP4X(23, -1, -1, 7,
         {
           char* cd = sm + L_COR + (t < 224 ? (t >> 5) * 1024 + (t & 31) * 16 : 7168 + (t & 31) * 16);
           *(u32x4*)cd = corr;
           *(u32x4*)(cd + 512) = ((u32x4){0u, 0u, 0u, 0u});
         }, , , )
  WAITG(14)
  asm volatile("" : "+v"(stg));
  asm volatile("" : "+a"(acc[0]), "+a"(acc[1]), "+a"(acc[2]), "+a"(acc[3]), "+a"(acc[4]), "+a"(acc[5]), "+a"(acc[6]));
#pragma nounroll
  for (int spins = 0; !__all(stg[1] == tag) && spins < SPIN_CAP; ++spins) {
    __builtin_amdgcn_s_sleep(1);
    asm volatile("global_load_dwordx2 %0, %1, %2 sc1\n\ts_waitcnt vmcnt(0)" : "=&v"(stg) : "v"(sto), "s"(stp) : "memory");
  }
  SCHED_FENCE();
  STEP(27, -1, -1, 14, 4)
  bf16x8 tl[2][4];
  u32x4 at[3];
#define RDT(m, k_) { if ((k_) == 3) tl[(m) & 1][k_] = *(const bf16x8*)(lsrc + L_COR + (m) * 1024); else tl[(m) & 1][k_] = TRF(29 + (k_), m); }
  const float mb = lo2f(stg[0]), sd = hi2f(stg[0]);
  float rs2;
  {
    MM(28, 0) RA(at[0], 29) SCHED_FENCE();
    MM(28, 1) RDT(0, 0) RDT(0, 1) SCHED_FENCE();
    MM(28, 2) RA(at[1], 30) WDM(17) SCHED_FENCE();
    MM(28, 3) RDT(0, 2) RDT(0, 3) WDM(18) SCHED_FENCE();
    MM(28, 4) RA(at[2], 31) WDM(19) SCHED_FENCE();
    MM(28, 5)
    const float rs = __builtin_amdgcn_rcpf(sd);
    rs2 = rs * rs;
    SCHED_FENCE();
    MM(28, 6) SCHED_FENCE();
  }
  STAMP(12);
  const u32x4 cb4 = {pk2(-mb, -mb), pk2(-mb, sd), pk2(sd, sd), 0u};
  const unsigned hoff = WS_HHF + ((unsigned)st * 112 + cbk * 7) * 1024;
  u32x4 eo;
#define EPJ(m, j)                                                                     \
  {                                                                                   \
    float u0 = acc[m][8 + 2 * (j)], u1 = acc[m][8 + 2 * (j) + 1];                     \
    asm volatile("" : "+v"(u0), "+v"(u1));                                            \
    float e0 = relu(acc[m][2 * (j)]) * u0 * rs2;                                      \
    float e1 = relu(acc[m][2 * (j) + 1]) * u1 * rs2;                                  \
    eo[j] = pk2(e0, e1);                                                              \
  }
#define EST(m) BST128(eo, voff + (m) * 1024, hoff);
#define MT(m, k_, bop) acc[m] = __builtin_amdgcn_mfma_f32_32x32x16_bf16(tl[(m) & 1][k_], __builtin_bit_cast(bf16x8, bop), acc[m], 0, 0, 0);
#define TAIL(m)                                                                                      \
  {                                                                                                  \
    MT(m, 0, at[0]) SCHED_FENCE();                                                                   \
    if ((m) < 6) { RDT((m) + 1, 0) RDT((m) + 1, 1) }                                                 \
    SCHED_FENCE();                                                                                   \
    MT(m, 1, at[1]) SCHED_FENCE();                                                                   \
    if ((m) < 6) { RDT((m) + 1, 2) RDT((m) + 1, 3) }                                                 \
    if ((m) > 0) EPJ((m) > 0 ? (m) - 1 : 0, 0)                                                       \
    SCHED_FENCE();                                                                                   \
    MT(m, 2, at[2]) SCHED_FENCE();                                                                   \
    if ((m) > 0) { EPJ((m) > 0 ? (m) - 1 : 0, 1) EPJ((m) > 0 ? (m) - 1 : 0, 2) }                     \
    SCHED_FENCE();                                                                                   \
    MT(m, 3, cb4) SCHED_FENCE();                                                                     \
    if ((m) > 0) { EPJ((m) > 0 ? (m) - 1 : 0, 3) EST((m) > 0 ? (m) - 1 : 0) }                        \
    SCHED_FENCE();                                                                                   \
  }
  TAIL(0) TAIL(1) TAIL(2) TAIL(3) TAIL(4) TAIL(5) TAIL(6)
  EPJ(6, 0) EPJ(6, 1) EPJ(6, 2) EPJ(6, 3) EST(6)
  asm volatile("s_waitcnt vmcnt(0)" ::: "memory");
  BST32(tag, ((st * 16 + cbk) * 64) | IF_(lane == 0), WS_FL2);
  STAMP(13);
  SCHED_FENCE();

  const unsigned dso = WS_WDF + ((unsigned)(2 * nbk) * 112 + 28 * w) * 1024;
#pragma unroll
  for (int r = 0; r < 36; ++r) wr2[r] = BLD128(voff, dso + (r & 1) * (112 * 1024) + (10 + (r >> 1)) * 1024);
  const int fn = w >> 1, fs = w & 1;
  const int ncol = 64 * nbk + 32 * fn;
  const int srow = 64 * sbk + 32 * fs + (lane & 31);
  u32 xr[8];
#pragma unroll
  for (int i = 0; i < 8; ++i)
    xr[i] = BLD32(((4 * h) * S + (lane & 31)) * 4, WS_X1P + (((ncol >> 1) + (i >> 2) * 8 + (i & 3)) * S + 64 * sbk + 32 * fs) * 4);
  SCHED_FENCE();
#pragma nounroll
  for (int spins = 0; spins < SPIN_CAP; ++spins) {
    const u32 f = BLD32(((2 * sbk + (lane & 1)) * 16 + 4 * w + ((lane >> 1) & 3)) * 64, WS_FL2);
    if (__all(f == tag)) break;
    __builtin_amdgcn_s_sleep(1);
  }
  __builtin_amdgcn_fence(__ATOMIC_ACQUIRE, "wavefront");
  STAMP(14);
  SCHED_FENCE();
  const unsigned hs = WS_HHF + ((unsigned)(2 * sbk) * 112 + 28 * w) * 1024;
  constexpr int WIN = 16;
  u32x4 hb[WIN][2];
#define HLD(i)                                                  \
  {                                                             \
    hb[(i) % WIN][0] = BLD128(voff, hs + (i) * 1024);           \
    hb[(i) % WIN][1] = BLD128(voff, hs + (112 + (i)) * 1024);   \
  }
#pragma unroll
  for (int i = 0; i < WIN; ++i) HLD(i)
  SCHED_FENCE();
  f32x16 ac2[2][2];
#pragma unroll
  for (int a = 0; a < 2; ++a)
#pragma unroll
    for (int c = 0; c < 2; ++c)
#pragma unroll
      for (int r = 0; r < 16; ++r) ac2[a][c][r] = 0.f;
  bf16x8 wl2[2][2];
  wl2[0][0] = *(const bf16x8*)(lsrc + wd_lds_off(0, w));
  wl2[0][1] = *(const bf16x8*)(lsrc + wd_lds_off(1, w));
#pragma unroll
  for (int i = 0; i < 28; ++i) {
    if (i + 1 < 10) {
      wl2[(i + 1) & 1][0] = *(const bf16x8*)(lsrc + wd_lds_off(2 * (i + 1), w));
      wl2[(i + 1) & 1][1] = *(const bf16x8*)(lsrc + wd_lds_off(2 * (i + 1) + 1, w));
    }
#pragma unroll
    for (int a = 0; a < 2; ++a)
#pragma unroll
      for (int c = 0; c < 2; ++c)
        ac2[a][c] = __builtin_amdgcn_mfma_f32_32x32x16_bf16(
            i < 10 ? wl2[i & 1][a] : __builtin_bit_cast(bf16x8, wr2[i < 10 ? 0 : 2 * (i - 10) + a]),
            __builtin_bit_cast(bf16x8, hb[i % WIN][c]), ac2[a][c], 0, 0, 0);
    if (i + WIN < 28) HLD(i + WIN)
    SCHED_FENCE();
  }
#define PARK(w_, j) (((j) - 1) * G_W + (w_) * 7168)
#pragma unroll
  for (int a = 0; a < 2; ++a)
#pragma unroll
    for (int c = 0; c < 2; ++c) {
      const int tile = a * 2 + c;
      if (tile != w) {
        const int j = (tile - w) & 3;
        char* dst = sm + PARK(w, j) + lane * 16;
#pragma unroll
        for (int r = 0; r < 4; ++r)
          *(f32x4*)(dst + r * 1024) =
              (f32x4){ac2[a][c][4 * r], ac2[a][c][4 * r + 1], ac2[a][c][4 * r + 2], ac2[a][c][4 * r + 3]};
      }
    }
  RAWBAR();
  f32x16 fin;
  if (w == 0) fin = ac2[0][0];
  else if (w == 1) fin = ac2[0][1];
  else if (w == 2) fin = ac2[1][0];
  else fin = ac2[1][1];
#pragma unroll
  for (int o = 1; o < 4; ++o) {
    const int ow = (w + o) & 3;
    const char* srcp = sm + PARK(ow, 4 - o) + lane * 16;
#pragma unroll
    for (int r = 0; r < 4; ++r) {
      f32x4 vv = *(const f32x4*)(srcp + r * 1024);
#pragma unroll
      for (int e = 0; e < 4; ++e) fin[4 * r + e] += vv[e];
    }
  }
  u32 pkd[8];
#pragma unroll
  for (int j = 0; j < 8; ++j) pkd[j] = pk2(fin[2 * j], fin[2 * j + 1]);
  u16* orow = out + (size_t)srow * D + ncol + 8 * h;
#pragma unroll
  for (int kk = 0; kk < 2; ++kk) {
    auto r0 = __builtin_amdgcn_permlane32_swap(pkd[4 * kk], pkd[4 * kk + 2], false, false);
    auto r1 = __builtin_amdgcn_permlane32_swap(pkd[4 * kk + 1], pkd[4 * kk + 3], false, false);
    u32 m4[4] = {r0[0], r1[0], r0[1], r1[1]};
    u32x4 o;
#pragma unroll
    for (int j = 0; j < 4; ++j) {
      u32 a = pk2(gbv[8 * kk + 2 * j] * lo2f(m4[j]), gbv[8 * kk + 2 * j + 1] * hi2f(m4[j]));
      o[j] = pk2(lo2f(xr[4 * kk + j]) + lo2f(a), hi2f(xr[4 * kk + j]) + hi2f(a));
    }
    *(u32x4*)(orow + 16 * kk) = o;
  }
  STAMP(15);
  STAMP_FLUSH();
}

__global__ void k_end(char* ws) {
  if (ws == nullptr) __builtin_trap();
}

extern "C" void kernel_launch(void* const* d_in, const int* in_sizes, int n_in, void* d_out, int out_size,
                              void* d_ws, size_t ws_size, hipStream_t stream) {
  (void)hipFuncSetAttribute((const void*)mega, hipFuncAttributeMaxDynamicSharedMemorySize, LDS_BYTES);
  mega<<<256, 256, LDS_BYTES, stream>>>((const u16*)d_in[0], (const u16*)d_in[1], (const u16*)d_in[2],
                                        (const u16*)d_in[3], (const u16*)d_in[4], (const u16*)d_in[5],
                                        (const u16*)d_in[7], (const u16*)d_in[8], (const u16*)d_in[9],
                                        (const u16*)d_in[10], (const u16*)d_in[11], (char*)d_ws, (u16*)d_out);
}
```

```cpp
#include <hip/hip_runtime.h>
#include <stdint.h>

typedef unsigned short u16;
typedef unsigned int u32;
typedef unsigned long long u64;
typedef __attribute__((ext_vector_type(8))) short bf16x8;
typedef __attribute__((ext_vector_type(4))) short s16x4;
typedef __attribute__((ext_vector_type(4))) float f32x4;
typedef __attribute__((ext_vector_type(2))) float f32x2;
typedef __attribute__((ext_vector_type(16))) float f32x16;
typedef __attribute__((ext_vector_type(4))) unsigned u32x4;
typedef __attribute__((ext_vector_type(2))) unsigned u32x2;
typedef __attribute__((ext_vector_type(2))) __bf16 bf2;
typedef __attribute__((address_space(3))) s16x4* lds_s4p;

#define DI __device__ __forceinline__

constexpr int S = 2048, D = 512, F = 1792;

DI float lo2f(u32 x) { return __uint_as_float(x << 16); }
DI float hi2f(u32 x) { return __uint_as_float(x & 0xffff0000u); }
DI u32 pk2(float a, float b) {
  f32x2 v = {a, b};
  return __builtin_bit_cast(u32, __builtin_convertvector(v, bf2));
}
DI float rb(float a) { return __uint_as_float(pk2(0.f, a)); }
DI f32x2 rb2(f32x2 a) { return (f32x2){rb(a[0]), rb(a[1])}; }
DI f32x2 up2(u32 x) { return (f32x2){lo2f(x), hi2f(x)}; }
DI u32 pk2v(f32x2 a) { return pk2(a[0], a[1]); }
DI float dot2(u32 a, u32 b, float c) {
  return __builtin_amdgcn_fdot2_f32_bf16(__builtin_bit_cast(bf2, a), __builtin_bit_cast(bf2, b), c, false);
}
template <int CTRL>
DI float dpp_add(float x) {
  return x + __uint_as_float(__builtin_amdgcn_update_dpp(0u, __float_as_uint(x), CTRL, 0xf, 0xf, false));
}
DI float red_16_32(float x) {
  auto a = __builtin_amdgcn_permlane16_swap(__float_as_uint(x), __float_as_uint(x), false, false);
  x = __uint_as_float(a[0]) + __uint_as_float(a[1]);
  auto b = __builtin_amdgcn_permlane32_swap(__float_as_uint(x), __float_as_uint(x), false, false);
  return __uint_as_float(b[0]) + __uint_as_float(b[1]);
}
DI float red_8_16_32(float x) { return red_16_32(dpp_add<0x128>(x)); }
DI float red_2_64(float x) {
  x = dpp_add<0x4E>(x);
  x = dpp_add<0x124>(x);
  return red_8_16_32(x);
}
DI float wave_sum(float x) { return red_2_64(dpp_add<0xB1>(x)); }
DI float relu(float x) {
  float r;
  asm("v_max_f32 %0, 0, %1" : "=v"(r) : "v"(x));
  return r;
}

#define SCHED_FENCE() __builtin_amdgcn_sched_barrier(0)
#define RAWBAR()                                          \
  do {                                                    \
    __builtin_amdgcn_sched_barrier(0);                    \
    asm volatile("s_waitcnt lgkmcnt(0)" ::: "memory");    \
    __builtin_amdgcn_s_barrier();                         \
    asm volatile("" ::: "memory");                        \
    __builtin_amdgcn_sched_barrier(0);                    \
  } while (0)
#define DRAINBAR()                                                 \
  do {                                                             \
    __builtin_amdgcn_sched_barrier(0);                             \
    asm volatile("s_waitcnt vmcnt(0) lgkmcnt(0)" ::: "memory");    \
    __builtin_amdgcn_s_barrier();                                  \
    asm volatile("" ::: "memory");                                 \
    __builtin_amdgcn_sched_barrier(0);                             \
  } while (0)

constexpr u32 WS_EPOCH = 0;
constexpr u32 WS_FLA = 4096;
constexpr u32 WS_FLW = WS_FLA + 256 * 64;
constexpr u32 WS_FL1 = WS_FLW + 256 * 64;
constexpr u32 WS_FL2 = WS_FL1 + 256 * 64;
constexpr u32 WS_MODG = WS_FL2 + 1024 * 64;
constexpr u32 WS_ST1G = WS_MODG + 4 * 3072 * 8;
constexpr u32 WS_ST2G = WS_ST1G + 2048 * 8;
constexpr u32 WS_CSP = WS_ST2G + 2048 * 8;
constexpr u32 WS_URT = 1u << 20;
constexpr u32 WS_QT2 = WS_URT + 2u * S * D;
constexpr u32 WS_X1P = WS_QT2 + 2u * S * D;
constexpr u32 WS_X1S = WS_X1P + 2u * S * D;
constexpr u32 WS_W1F = WS_X1S + 2u * S * D;
constexpr u32 W1F_CB = 32 * 7 * 1024;
constexpr u32 WS_WDF = WS_W1F + 16 * W1F_CB;
constexpr u32 WS_HHF = WS_WDF + 2u * F * D;
constexpr u32 WS_END = WS_HHF + 2u * S * F;
static_assert(WS_CSP + 16 * 8 * 224 * 16 <= WS_URT, "ws layout");

constexpr int L_CS = 4160;
constexpr int L_FC = 0;
constexpr int L_TU = 66560;
constexpr int L_TQ = L_TU + 8192;
constexpr int L_WSTW = 16 * 288 + 16 * 144;
constexpr int L_WST = L_TQ + 8192;
constexpr int L_SCR = L_WST + 4 * L_WSTW;
constexpr int L_U = 114688;
constexpr int L_PART = L_U + 2 * 4608;
constexpr int L_MV = L_PART + 36864;
constexpr int L_RED = L_U;
constexpr int G_W = 28672;
constexpr int L_A = 4 * G_W;
constexpr int G_A = 16384;
constexpr int L_COR = L_A + 2 * G_A;
constexpr int LDS_BYTES = 163840;
static_assert(L_SCR + 4096 <= L_U && L_MV + 1024 <= LDS_BYTES, "lds layout");

constexpr int SPIN_CAP = 30000;
#define IF_(c) ((c) ? 0u : 0x80000000u)

#define BLD128(off, soff) __builtin_amdgcn_raw_buffer_load_b128(wsr, (off), (soff), 16)
#define BLD64(off, soff) __builtin_amdgcn_raw_buffer_load_b64(wsr, (off), (soff), 16)
#define BLD32(off, soff) __builtin_amdgcn_raw_buffer_load_b32(wsr, (off), (soff), 16)
#define BST128(v, off, soff) __builtin_amdgcn_raw_buffer_store_b128((v), wsr, (unsigned)(off) + (unsigned)(soff), 0, 16)
#define BST64(v, off, soff) __builtin_amdgcn_raw_buffer_store_b64((v), wsr, (off), (soff), 16)
#define BST32(v, off, soff) __builtin_amdgcn_raw_buffer_store_b32((v), wsr, (off), (soff), 16)
#define GDMA(voff, sbase, ldsa, imm)                                                                  \
  asm volatile("s_mov_b32 m0, %2\n\ts_nop 0\n\tglobal_load_lds_dwordx4 %0, %1 offset:" #imm " sc1"    \
               ::"v"(voff), "s"(sbase), "s"(ldsa) : "memory")

#define GDMB(voff, sbase, imm)                                                                        \
  asm volatile("global_load_lds_dwordx4 %0, %1 offset:" #imm " sc1" ::"v"(voff), "s"(sbase) : "memory")

DI constexpr int conv_n(int i) {
  return i < 8 ? 0 : i < 15 ? 1 : i < 21 ? 2 : i < 26 ? 3 : i < 30 ? 4 : i == 30 ? 5 : i == 31 ? 7 : i < 34 ? 5 : 6;
}
DI constexpr int conv_d(int i) {
  return i < 8 ? i : i < 15 ? i - 8 : i < 21 ? i - 15 : i < 26 ? i - 21 : i < 30 ? i - 26 : i < 32 ? 0 : i < 34 ? i - 31 : i - 34;
}
DI constexpr int wd_lds_off(int q, int w) { return (q / 7) * G_W + w * 7168 + (q % 7) * 1024; }
constexpr int WD_EARLY = 18;

__global__ __launch_bounds__(256, 1) __attribute__((amdgpu_waves_per_eu(1, 1))) void mega(
    const u16* __restrict__ q, const u16* __restrict__ k, const u16* __restrict__ v, const u16* __restrict__ shk,
    const u16* __restrict__ kf, const u16* __restrict__ ssmD, const u16* __restrict__ wg, const u16* __restrict__ wu,
    const u16* __restrict__ wd, const u16* __restrict__ cc, const u16* __restrict__ wmod, char* __restrict__ ws,
    u16* __restrict__ out) {
  extern __shared__ __attribute__((aligned(16))) char sm[];
  const int b = blockIdx.x, t = threadIdx.x;
  const int w = __builtin_amdgcn_readfirstlane(t >> 6), lane = t & 63;
  const int xcd = b & 7, bi = b >> 3;
  const auto wsr = __builtin_amdgcn_make_buffer_rsrc(ws, 0, WS_END, 0x00020000);
  const int p = xcd * 32 + bi;
  const bool has_w = bi < 28;
  const int it = xcd * 28 + (has_w ? bi : 0);
  const int slab = it >> 3, kc = it & 7;
  const int rbk = (xcd & 1) * 8 + (bi & 7);
  const int cbk = (xcd >> 1) * 4 + (bi >> 3);
  const int sbk = xcd * 4 + (bi & 3);
  const int nbk = bi >> 2;
  const int g = lane >> 4, h = lane >> 5, qq = (lane & 15) >> 2, pp = lane & 3, l15 = lane & 15;

  const int st = rbk * 4 + w;
  const char* wsrc = ws + WS_W1F + (size_t)cbk * W1F_CB + w * 7168;
  const char* asrc = ws + WS_X1S + (size_t)(8 * w) * (S * 4) + rbk * 512;
  const char* dsrc = ws + WS_WDF + ((size_t)(2 * nbk + (w >> 1)) * 112 + 28 * w) * 1024;
  const char* dsrc1 = ws + WS_WDF + ((size_t)(2 * nbk + 1 - (w >> 1)) * 112 + 28 * w) * 1024;
  const unsigned lbase = (unsigned)(uintptr_t)(__attribute__((address_space(3))) char*)sm;
  const unsigned ldw = lbase + w * 7168;
  const unsigned lda = lbase + L_A + w * 4096;
  const unsigned voff = lane * 16;
  const unsigned voff4 = voff + 4096;
  unsigned aof[4];
#pragma unroll
  for (int j = 0; j < 4; ++j) aof[j] = (((lane >> 5) * (S * 4) + (lane & 31) * 16) ^ ((j & 2) ? 64 : 0)) + j * (2 * S * 4 - 1024);

#define VW(g, j)                                                                                  \
  {                                                                                               \
    if ((j) == 0) GDMA(voff, wsrc + (g) * G_W, ldw + ((g) & 3) * G_W, 0);                         \
    if ((j) == 1) GDMB(voff, wsrc + (g) * G_W, 1024);                                             \
    if ((j) == 2) GDMB(voff, wsrc + (g) * G_W, 2048);                                             \
    if ((j) == 3) GDMB(voff, wsrc + (g) * G_W, 3072);                                             \
    if ((j) == 4) GDMA(voff4, wsrc + (g) * G_W, ldw + ((g) & 3) * G_W + 4096, 0);                 \
    if ((j) == 5) GDMB(voff4, wsrc + (g) * G_W, 1024);                                            \
    if ((j) == 6) GDMB(voff4, wsrc + (g) * G_W, 2048);                                            \
  }
#define VA(g, j)                                                                                            \
  {                                                                                                         \
    if ((j) == 0) GDMA(aof[0], asrc + (size_t)((g) * 32) * (S * 4), lda + ((g) % 3) * G_A, 0);              \
    if ((j) == 1) GDMB(aof[1], asrc + (size_t)((g) * 32) * (S * 4), 1024);                                  \
    if ((j) == 2) GDMB(aof[2], asrc + (size_t)((g) * 32) * (S * 4), 2048);                                  \
    if ((j) == 3) GDMB(aof[3], asrc + (size_t)((g) * 32) * (S * 4), 3072);                                  \
  }
#define VWALL(g) { VW(g, 0) VW(g, 1) VW(g, 2) VW(g, 3) VW(g, 4) VW(g, 5) VW(g, 6) }
#define VAALL(g) { VA(g, 0) VA(g, 1) VA(g, 2) VA(g, 3) }
#define WDM(q_) { if ((q_) >= 0 && (q_) < 20) GDMA(voff, (((q_) & 1) ? dsrc1 : dsrc) + ((q_) >> 1) * 1024, lbase + wd_lds_off((q_) < 0 ? 0 : (q_), w), 0); }

  u32x4 wr2[36];
#define WDR(r) asm volatile("global_load_dwordx4 %0, %1, %2 sc1" : "=a"(wr2[r]) : "v"(voff), "s"((((r) & 1) ? dsrc1 : dsrc) + (10 + ((r) >> 1)) * 1024) : "memory");
  unsigned fwo;
  {
    const int sl0 = (7 * cbk) >> 2, sl1 = (7 * cbk + 6) >> 2;
    const int sl_ = sl0 + (lane >> 3) < sl1 ? sl0 + (lane >> 3) : sl1;
    const int it_ = lane >= 32 ? (7 * w + (lane < 39 ? lane - 32 : 0)) * 8 + nbk : sl_ * 8 + (lane & 7);
    fwo = WS_FLW + ((it_ % 28) * 8 + it_ / 28) * 64;
  }

  u32 tag;
  u32x4 ka[2], kb[2], wl[6];
  if (t == 0) *(u32*)(sm + L_SCR + 2048) = 0u;
  {
    const int sb = xcd * 4 + (bi >> 3), db = bi & 7;
    const int rp = t >> 3, ci = t & 7;
    const int s0 = 64 * sb + 2 * rp, ch0 = 64 * db + 8 * ci;
    const u32 ep = BLD32(0, WS_EPOCH);
    u32x4 kr[5], vr[2], wr[4], qr[2];
#pragma unroll
    for (int i = 0; i < 5; ++i) {
      int s = s0 - 3 + i;
      kr[i] = *(const u32x4*)(k + (size_t)(s < 0 ? 0 : s) * D + ch0);
    }
#pragma unroll
    for (int i = 0; i < 2; ++i) vr[i] = *(const u32x4*)(v + (size_t)(s0 + i) * D + ch0);
#pragma unroll
    for (int i = 0; i < 4; ++i) wr[i] = *(const u32x4*)(shk + (size_t)ch0 * 4 + i * 8);
#pragma unroll
    for (int i = 0; i < 2; ++i) qr[i] = *(const u32x4*)(q + (size_t)(s0 + i) * D + ch0);
    u32 cpf;
    {
      u64 c0;
      int r0_;
      asm volatile("s_getpc_b64 %0\n\ts_mov_b32 %1, _Z5k_endPc@rel32@lo+4" : "=s"(c0), "=s"(r0_));
      const unsigned off = (unsigned)(bi * 20 + (t & 31)) * 64;
      const char* cp_ = (const char*)(c0 & ~63ull) + (off + 64 <= (unsigned)r0_ ? off : 0u);
      asm volatile("global_load_dword %0, %1, off" : "=v"(cpf) : "v"(cp_) : "memory");
    }
    SCHED_FENCE();
    u32* Tu = (u32*)(sm + L_TU);
    u32* Tq = (u32*)(sm + L_TQ);
    const int x = 31 - rp;
    const char* kfp = (const char*)(kf + (size_t)(2 * p) * S);
    unsigned kfa = (t > 0 ? t - 1 : 0) * 16, kfb = t * 16;
    asm volatile("" : "+v"(kfa), "+v"(kfb));
    const int r0 = 8 * b + 2 * w;
    const int mi = b < 192 ? b : 0;
    const int mslab = mi % 48, mq = mi / 48;
    unsigned mo = ((unsigned)(128 * mq + 4 * rp) * (6 * D) + 64 * mslab + 8 * ci) * 2, xo = ((unsigned)r0 * D + lane * 8) * 2;
    asm volatile("" : "+v"(mo), "+v"(xo));
    asm volatile("s_waitcnt vmcnt(0)" : "+v"(cpf), "+v"(wr[3]), "+v"(vr[1]), "+v"(kr[4]), "+v"(qr[1])::"memory");
    SCHED_FENCE();
    u32x4 xr[2], wv[4];
#pragma unroll
    for (int i = 0; i < 4; ++i) wv[i] = *(const u32x4*)((const char*)wmod + i * (6 * D * 2) + mo);
    const u32x2 cv = *(const u32x2*)(cc + 128 * mq + 4 * rp);
#pragma unroll
    for (int ch = 0; ch < 2; ++ch) {
      ka[ch] = *(const u32x4*)(kfp + ch * (S * 2) + kfa);
      kb[ch] = *(const u32x4*)(kfp + ch * (S * 2) + kfb);
    }
#pragma unroll
    for (int i = 0; i < 2; ++i) xr[i] = *(const u32x4*)((const char*)q + i * (D * 2) + xo);
    SCHED_FENCE();
    tag = __builtin_amdgcn_readfirstlane(ep) + 1u;
#pragma unroll
    for (int i = 0; i < 3; ++i) {
      const bool neg = (s0 - 3 + i) < 0;
#pragma unroll
      for (int j = 0; j < 4; ++j) kr[i][j] = neg ? 0u : kr[i][j];
    }
#pragma unroll
    for (int j = 0; j < 8; ++j) {
      const u32 w01 = wr[j >> 1][(j & 1) * 2], w23 = wr[j >> 1][(j & 1) * 2 + 1];
      const float w0 = lo2f(w01), w1 = hi2f(w01), w2 = lo2f(w23), w3 = hi2f(w23);
      float kk[5];
#pragma unroll
      for (int i = 0; i < 5; ++i) kk[i] = (j & 1) ? hi2f(kr[i][j >> 1]) : lo2f(kr[i][j >> 1]);
      f32x2 a = rb2((f32x2){kk[0], kk[1]} * w3);
      a = rb2(a + rb2((f32x2){kk[1], kk[2]} * w2));
      a = rb2(a + rb2((f32x2){kk[2], kk[3]} * w1));
      a = rb2(a + rb2((f32x2){kk[3], kk[4]} * w0));
      const float v0 = (j & 1) ? hi2f(vr[0][j >> 1]) : lo2f(vr[0][j >> 1]);
      const float v1 = (j & 1) ? hi2f(vr[1][j >> 1]) : lo2f(vr[1][j >> 1]);
      const u32 u = pk2(a[1] * v1, a[0] * v0);
      Tu[(8 * ci + j) * 32 + ((((x >> 2) ^ ci) << 2) | (x & 3))] = u;
    }
#pragma unroll
    for (int j = 0; j < 4; ++j) {
      const int xs = 2 * rp;
      u32x2 val = {qr[0][j], qr[1][j]};
      *(u32x2*)&Tq[(4 * ci + j) * 64 + ((((xs >> 2) ^ (2 * ci)) << 2) | (xs & 3))] = val;
    }
    RAWBAR();
    {
      const int tbase = S - 64 * (sb + 1);
#pragma unroll
      for (int i = 0; i < 2; ++i) {
        const int idx = t + 256 * i;
        const int ch = idx >> 3, c8 = idx & 7;
        u32x4 val = *(const u32x4*)&Tu[ch * 32 + ((c8 ^ (ch >> 3)) << 2)];
        BST128(val, ((64 * db + ch) * S + tbase + c8 * 8) * 2, WS_URT);
        const int pr = idx >> 4, c16 = idx & 15;
        u32x4 qv = *(const u32x4*)&Tq[pr * 64 + ((c16 ^ (2 * (pr >> 2))) << 2)];
        BST128(qv, ((32 * db + pr) * S + 64 * sb + c16 * 4) * 4, WS_QT2);
      }
    }
    SCHED_FENCE();
    {
      float acc[8];
#pragma unroll
      for (int j = 0; j < 8; ++j) acc[j] = 0.f;
#pragma unroll
      for (int i = 0; i < 4; ++i) {
        const float xx = (i & 1) ? hi2f(cv[i >> 1]) : lo2f(cv[i >> 1]);
        const float sl = rb(xx * __builtin_amdgcn_rcpf(1.f + __expf(-xx)));
#pragma unroll
        for (int j = 0; j < 4; ++j) {
          acc[2 * j] += sl * lo2f(wv[i][j]);
          acc[2 * j + 1] += sl * hi2f(wv[i][j]);
        }
      }
#pragma unroll
      for (int j = 0; j < 8; ++j) acc[j] = red_8_16_32(acc[j]);
      if (lane < 8) {
        float* red = (float*)(sm + L_SCR) + w * 64 + lane * 8;
        *(f32x4*)red = (f32x4){acc[0], acc[1], acc[2], acc[3]};
        *(f32x4*)(red + 4) = (f32x4){acc[4], acc[5], acc[6], acc[7]};
      }
    }
    DRAINBAR();
    BST32(tag, (b * 64) | IF_(t == 0), WS_FLA);
    {
      const float* red = (const float*)(sm + L_SCR);
      const float r = (red[lane] + red[64 + lane]) + (red[128 + lane] + red[192 + lane]);
      u32x2 gr = {__float_as_uint(r), tag};
      BST64(gr, ((mq * 3072 + 64 * mslab + lane) * 8) | IF_(w == 1 && b < 192), WS_MODG);
    }
    {
      const unsigned wo1 = ((unsigned)(32 * (w >> 1) + (lane >> 3)) * F + 8 * (lane & 7)) * 2;
      const unsigned wo2 = ((unsigned)(16 * w + (lane >> 3)) * D + 8 * (lane & 7)) * 2;
      const char* wgb = (const char*)(((w & 1) ? wu : wg) + (size_t)(64 * kc) * F + 64 * slab);
      const char* wdb = (const char*)(wd + (size_t)(64 * slab) * D + 64 * kc);
#pragma unroll
      for (int i = 0; i < 4; ++i) wl[i] = *(const u32x4*)(wgb + i * (8 * F * 2) + wo1);
      wl[4] = *(const u32x4*)(wdb + wo2);
      wl[5] = *(const u32x4*)(wdb + 8 * D * 2 + wo2);
    }
    SCHED_FENCE();
    {
      float mean[2], var[2];
#pragma unroll
      for (int i = 0; i < 2; ++i) {
        float a = 0.f;
#pragma unroll
        for (int j = 0; j < 4; ++j) a = dot2(xr[i][j], 0x3f803f80u, a);
        mean[i] = wave_sum(a) * (1.f / D);
      }
#pragma unroll
      for (int i = 0; i < 2; ++i) {
        f32x2 a = {0.f, 0.f};
#pragma unroll
        for (int j = 0; j < 4; ++j) {
          const f32x2 d = up2(xr[i][j]) - mean[i];
          a += d * d;
        }
        var[i] = wave_sum(a[0] + a[1]) * (1.f / D);
      }
      const float m_ = lane == 0 ? mean[0] : mean[1], v_ = lane == 0 ? var[0] : var[1];
      const float sd = __builtin_amdgcn_sqrtf(rb(rb(v_) + 1e-5f));
      u32x2 gr = {pk2(m_, sd), tag};
      BST64(gr, ((r0 + lane) * 8) | IF_(lane < 2), WS_ST1G);
    }
  }


  float gbv[16];
  u32x4 wfr[4];
  const float* mv = (const float*)(sm + L_MV);
  {
    const u32 dpair = ((const u32*)ssmD)[p];
#pragma unroll
    for (int ch = 0; ch < 2; ++ch) {
      u32 fv[8] = {ka[ch][0], ka[ch][1], ka[ch][2], ka[ch][3], kb[ch][0], kb[ch][1], kb[ch][2], kb[ch][3]};
#pragma unroll
      for (int i = 0; i < 4; ++i) fv[i] = t > 0 ? fv[i] : 0u;
      u32 od[7];
#pragma unroll
      for (int i = 0; i < 7; ++i) od[i] = __builtin_amdgcn_alignbit(fv[i + 1], fv[i], 16);
      char* base = sm + L_FC + ch * (8 * L_CS) + (t + 1) * 16;
#pragma unroll
      for (int r = 0; r < 8; ++r) {
        const int sg = r + 1;
        u32x4 val;
        if (sg & 1) {
          const int k0 = (sg - 1) / 2;
          val = (u32x4){od[k0], od[k0 + 1], od[k0 + 2], od[k0 + 3]};
        } else {
          const int k0 = sg / 2;
          val = (u32x4){fv[k0], fv[k0 + 1], fv[k0 + 2], fv[k0 + 3]};
        }
        *(u32x4*)(base + r * L_CS) = val;
      }
    }
    {
      const u32x4 z = {0, 0, 0, 0};
      if (t < 16) *(u32x4*)(sm + L_FC + t * L_CS) = z;
      if (t >= 64 && t < 128) *(u32x4*)(sm + L_U + ((t >> 5) & 1) * 4608 + 4096 + (t & 31) * 16) = z;
    }
#pragma nounroll
    for (int spins = 0; spins < SPIN_CAP; ++spins) {
      const u32 f = BLD32(((((lane & 3) * 8 + xcd) * 8) + ((lane & 31) >> 2)) * 64, WS_FLA);
      if (__all(f == tag)) break;
      __builtin_amdgcn_s_sleep(1);
    }
    __builtin_amdgcn_fence(__ATOMIC_ACQUIRE, "wavefront");
    u32x4 uu[2], qp[2];
#pragma unroll
    for (int ch = 0; ch < 2; ++ch) uu[ch] = BLD128(((2 * p + ch) * S + t * 8) * 2, WS_URT);
#pragma unroll
    for (int i = 0; i < 2; ++i) qp[i] = BLD128((p * S + 1024 * i + 4 * t) * 4, WS_QT2);
    SCHED_FENCE();
    {
      char* wst = sm + L_WST + w * L_WSTW;
      {
        char* d1 = wst + (lane >> 3) * 144 + (lane & 7) * 16;
#pragma unroll
        for (int i = 0; i < 6; ++i) *(u32x4*)(d1 + i * (8 * 144)) = wl[i];
      }
      __builtin_amdgcn_fence(__ATOMIC_RELEASE, "wavefront");
      __builtin_amdgcn_wave_barrier();
      __builtin_amdgcn_fence(__ATOMIC_ACQUIRE, "wavefront");
      const int ks = 2 * kc + (w >> 1);
#pragma unroll
      for (int j = 0; j < 4; ++j) {
        const int ft = 4 * slab + j;
        const char* a0 = wst + (8 * g + qq) * 144 + j * 32 + pp * 8;
        s16x4 lo = __builtin_amdgcn_ds_read_tr16_b64_v4i16((lds_s4p)(a0));
        s16x4 hi = __builtin_amdgcn_ds_read_tr16_b64_v4i16((lds_s4p)(a0 + 4 * 144));
        u32x2 l2 = __builtin_bit_cast(u32x2, lo), h2 = __builtin_bit_cast(u32x2, hi);
        wfr[j] = (u32x4){l2[0], l2[1], h2[0], h2[1]};
        BST128(wfr[j], (lane * 16) | IF_(has_w), WS_W1F + (ft / 7) * W1F_CB + ks * 14336 + (2 * (ft % 7) + (w & 1)) * 1024);
      }
#pragma unroll
      for (int j = 0; j < 2; ++j) {
        const char* a0 = wst + 32 * 144 + (8 * h + qq) * 144 + j * 64 + (g & 1) * 32 + pp * 8;
        s16x4 lo = __builtin_amdgcn_ds_read_tr16_b64_v4i16((lds_s4p)(a0));
        s16x4 hi = __builtin_amdgcn_ds_read_tr16_b64_v4i16((lds_s4p)(a0 + 4 * 144));
        u32x2 l2 = __builtin_bit_cast(u32x2, lo), h2 = __builtin_bit_cast(u32x2, hi);
        u32x4 o = {l2[0], l2[1], h2[0], h2[1]};
        BST128(o, (lane * 16) | IF_(has_w), WS_WDF + ((2 * kc + j) * 112 + 4 * slab + w) * 1024);
      }
    }
    SCHED_FENCE();
    const int cch = w >> 1, c0 = 4 * (w & 1);
    const char* fb = sm + L_FC + cch * (8 * L_CS) + (l15 & 7) * L_CS + 2 * ((l15 & 8) + 8 * g) + 64 * c0;
    const char* gb = sm + L_U + cch * 4608 + 2 * (2032 - 256 * 7 - 16 * l15 + 8 * g) + 64 * c0;
#pragma unroll
    for (int ch = 0; ch < 2; ++ch) *(u32x4*)(sm + L_U + ch * 4608 + t * 16) = uu[ch];
    RAWBAR();
    const int me = t < 64 ? 4 * D + 64 * kc + t
                 : t < 128 ? 3 * D + 64 * kc + (t - 64)
                 : t < 192 ? 5 * D + 64 * nbk + (t - 128)
                           : ((((t - 192) >> 1) & 3) == 3 ? 4 : (((t - 192) >> 1) & 3)) * D + 2 * p + (t & 1);
    u32x4 sg[4];
    u32 fw;
    u32x2 mg[4];
    f32x4 acc[8];
    {
      bf16x8 Fr[2][8], Gr[2][8];
#pragma unroll
      for (int n = 0; n < 8; ++n) Gr[0][n] = *(const bf16x8*)(gb + 512 * (7 - n));
#pragma unroll
      for (int n = 0; n < 8; ++n) Fr[0][n] = *(const bf16x8*)(fb + 512 * n);
      SCHED_FENCE();
#pragma unroll
      for (int m = 0; m < 8; ++m) acc[m] = (f32x4){0.f, 0.f, 0.f, 0.f};
#pragma unroll
      for (int c = 0; c < 4; ++c) {
        const int cur = c & 1, nxt = cur ^ 1;
#pragma unroll
        for (int i = 0; i < 36; ++i) {
          const int n = conv_n(i), dl = conv_d(i);
          acc[n + dl] = __builtin_amdgcn_mfma_f32_16x16x32_bf16(Fr[cur][n], Gr[cur][dl], acc[n + dl], 0, 0, 0);
          if (c < 3 && (i & 1) && i < 32) {
            const int j = i >> 1;
            if (j == 0) Fr[nxt][0] = *(const bf16x8*)(fb + 64 * (c + 1));
            else if (j <= 8) Gr[nxt][j - 1] = *(const bf16x8*)(gb + 512 * (8 - j) + 64 * (c + 1));
            else Fr[nxt][j - 8] = *(const bf16x8*)(fb + 512 * (j - 8) + 64 * (c + 1));
          }
          if (i & 1) SCHED_FENCE();
        }
        if (c == 0) {
          asm volatile("s_waitcnt vmcnt(0)" ::: "memory");
          u32 old = 0;
          if (lane == 0) old = __hip_atomic_fetch_add((u32*)(sm + L_SCR + 2048), 1u, __ATOMIC_RELAXED, __HIP_MEMORY_SCOPE_WORKGROUP);
          BST32(tag, (b * 64) | IF_(lane == 0 && old == 3), WS_FLW);
          SCHED_FENCE();
        }
        if (c == 1) {
#pragma unroll
          for (int i = 0; i < 2; ++i) {
            sg[2 * i] = BLD128((1024 * i + 4 * t) * 8, WS_ST1G);
            sg[2 * i + 1] = BLD128((1024 * i + 4 * t) * 8 + 16, WS_ST1G);
          }
#pragma unroll
          for (int i = 0; i < 4; ++i) mg[i] = BLD64(me * 8, WS_MODG + i * 3072 * 8);
          SCHED_FENCE();
        }
        if (c == 2) {
          fw = BLD32(fwo, 0);
          SCHED_FENCE();
        }
      }
    }
    {
      float* part = (float*)(sm + L_PART) + w * (8 * 288) + g * 72 + l15 * 4;
#pragma unroll
      for (int m = 0; m < 8; ++m) *(f32x4*)(part + m * 288) = acc[m];
    }
    asm volatile("" : "+v"(fw), "+v"(sg[0]), "+v"(sg[1]), "+v"(sg[2]), "+v"(sg[3]), "+v"(mg[0]), "+v"(mg[1]), "+v"(mg[2]), "+v"(mg[3]));
    if (__builtin_expect(!__all(fw == tag), 0)) {
#pragma nounroll
      for (int spins = 0; spins < SPIN_CAP; ++spins) {
        __builtin_amdgcn_s_sleep(1);
        fw = BLD32(fwo, 0);
        if (__all(fw == tag)) break;
      }
    }
    __builtin_amdgcn_fence(__ATOMIC_ACQUIRE, "wavefront");
    {
      bool ok = true;
#pragma unroll
      for (int i = 0; i < 4; ++i) ok = ok && mg[i][1] == tag && sg[i][1] == tag && sg[i][3] == tag;
      if (__builtin_expect(!__all(ok), 0)) {
#pragma nounroll
        for (int spins = 0; spins < SPIN_CAP; ++spins) {
#pragma unroll
          for (int i = 0; i < 4; ++i) mg[i] = BLD64(me * 8, WS_MODG + i * 3072 * 8);
#pragma unroll
          for (int i = 0; i < 2; ++i) {
            sg[2 * i] = BLD128((1024 * i + 4 * t) * 8, WS_ST1G);
            sg[2 * i + 1] = BLD128((1024 * i + 4 * t) * 8 + 16, WS_ST1G);
          }
          asm volatile("" : "+v"(sg[0]), "+v"(sg[1]), "+v"(sg[2]), "+v"(sg[3]), "+v"(mg[0]), "+v"(mg[1]), "+v"(mg[2]), "+v"(mg[3]));
          bool ok2 = true;
#pragma unroll
          for (int i = 0; i < 4; ++i) ok2 = ok2 && mg[i][1] == tag && sg[i][1] == tag && sg[i][3] == tag;
          if (__all(ok2)) break;
          __builtin_amdgcn_s_sleep(2);
        }
      }
    }
    SCHED_FENCE();
#pragma unroll
    for (int r = 0; r < WD_EARLY; ++r) WDR(r)
    SCHED_FENCE();
    {
      float val = rb((__uint_as_float(mg[0][0]) + __uint_as_float(mg[1][0])) + (__uint_as_float(mg[2][0]) + __uint_as_float(mg[3][0])));
      if (t < 64 || (t >= 192 && (t & 2))) val = rb(1.f + val);
      ((float*)(sm + L_MV))[t] = val;
    }
    f32x2 ud[8];
    {
      const f32x2 dd = up2(dpair);
#pragma unroll
      for (int i = 0; i < 2; ++i) {
        const u32x2 a0 = *(const u32x2*)(sm + L_U + (2044 - 1024 * i - 4 * t) * 2);
        const u32x2 a1 = *(const u32x2*)(sm + L_U + 4608 + (2044 - 1024 * i - 4 * t) * 2);
#pragma unroll
        for (int e = 0; e < 4; ++e) {
          const u32 w0 = a0[1 - (e >> 1)], w1 = a1[1 - (e >> 1)];
          const f32x2 uv = {(e & 1) ? lo2f(w0) : hi2f(w0), (e & 1) ? lo2f(w1) : hi2f(w1)};
          ud[4 * i + e] = rb2(uv * dd);
          asm volatile("" : "+v"(ud[4 * i + e]));
        }
      }
    }
    f32x2 qn[8];
#pragma unroll
    for (int i = 0; i < 8; ++i) {
      const u32 ms = sg[i >> 1][(i & 1) * 2];
      const float mb = lo2f(ms), rs = __builtin_amdgcn_rcpf(hi2f(ms));
      qn[i] = rb2(rb2(up2(qp[i >> 2][i & 3]) - mb) * rs);
      asm volatile("" : "+v"(qn[i]));
    }
    RAWBAR();
    const f32x2 sa = {mv[192], mv[193]}, oc = {mv[194], mv[195]}, ga = {mv[196], mv[197]}, ob = {mv[198], mv[199]};
    {
      const float* part = (const float*)(sm + L_PART) + (t >> 6) * 288 + (t & 3) * 72 + ((t >> 2) & 15) * 4;
#pragma unroll
      for (int i = 0; i < 2; ++i) {
        const f32x4 y0 = *(const f32x4*)(part + i * 4 * 288) + *(const f32x4*)(part + 2304 + i * 4 * 288);
        const f32x4 y1 = *(const f32x4*)(part + 2 * 2304 + i * 4 * 288) + *(const f32x4*)(part + 3 * 2304 + i * 4 * 288);
        u32x4 o, os;
#pragma unroll
        for (int e = 0; e < 4; ++e) {
          const f32x2 hh_ = rb2(rb2(qn[4 * i + e] * oc) + sa);
          const f32x2 z = rb2(rb2((f32x2){y0[e], y1[e]}) + ud[4 * i + e]);
          const f32x2 a = rb2(rb2(hh_ * z) * ga);
          const f32x2 x1 = rb2(up2(qp[i][e]) + a);
          o[e] = __builtin_amdgcn_perm(__float_as_uint(x1[1]), __float_as_uint(x1[0]), 0x07060302u);
          os[e] = pk2v(x1 * ob);
        }
        BST128(os, (p * S + 1024 * i + 4 * t) * 4, WS_X1S);
        BST128(o, (p * S + 1024 * i + 4 * t) * 4, WS_X1P);
      }
    }
  }
  SCHED_FENCE();

  DRAINBAR();
  BST32(tag, (b * 64) | IF_(t == 0), WS_FL1);
  SCHED_FENCE();
  VWALL(0) VWALL(1) VWALL(2)
  SCHED_FENCE();
  u32 f1;
  {
    const int fn = w >> 1;
#pragma unroll
    for (int i = 0; i < 4; ++i) {
      const f32x4 gq = *(const f32x4*)(mv + 128 + 32 * fn + 8 * h + (i >> 1) * 16 + (i & 1) * 4);
#pragma unroll
      for (int e = 0; e < 4; ++e) gbv[4 * i + e] = gq[e];
    }
    const float* mk = mv + 32 * (w >> 1) + 8 * g;
    const f32x4 sc0 = *(const f32x4*)(mk), sc1 = *(const f32x4*)(mk + 4);
    const f32x4 sb0 = *(const f32x4*)(mk + 64), sb1 = *(const f32x4*)(mk + 68);
    const u32 scv[4] = {pk2(sc0[0], sc0[1]), pk2(sc0[2], sc0[3]), pk2(sc1[0], sc1[1]), pk2(sc1[2], sc1[3])};
    const u32 sbv[4] = {pk2(sb0[0], sb0[1]), pk2(sb0[2], sb0[3]), pk2(sb1[0], sb1[1]), pk2(sb1[2], sb1[3])};
#pragma unroll
    for (int j = 0; j < 4; ++j) {
      float cs = 0.f, bs = 0.f;
#pragma unroll
      for (int e = 0; e < 4; ++e) {
        cs = dot2(wfr[j][e], scv[e], cs);
        bs = dot2(wfr[j][e], sbv[e], bs);
      }
      *(f32x2*)(sm + L_RED + ((w * 4 + j) * 64 + lane) * 8) = (f32x2){cs, bs};
    }
    RAWBAR();
    f1 = BLD32(t * 64, WS_FL1);
    SCHED_FENCE();
    const f32x2* red = (const f32x2*)(sm + L_RED) + (((h * 2 + (g & 1)) * 4 + w) * 64 + l15);
    f32x2 rr = (red[0] + red[16]) + (red[32] + red[48]);
    auto x0 = __builtin_amdgcn_permlane32_swap(__float_as_uint(rr[0]), __float_as_uint(rr[0]), false, false);
    auto x1 = __builtin_amdgcn_permlane32_swap(__float_as_uint(rr[1]), __float_as_uint(rr[1]), false, false);
    u32x4 o = {__float_as_uint(__uint_as_float(x0[0]) + __uint_as_float(x0[1])), tag,
               __float_as_uint(__uint_as_float(x1[0]) + __uint_as_float(x1[1])), tag};
    const int ft = 4 * slab + w;
    BST128(o, (lane * 16) | IF_(has_w && lane < 32), WS_CSP + (((ft / 7) * 8 + kc) * 224 + (ft % 7) * 32) * 16);
  }
#pragma nounroll
  for (int spins = 0; !__all(f1 == tag) && spins < SPIN_CAP; ++spins) {
    __builtin_amdgcn_s_sleep(1);
    f1 = BLD32(t * 64, WS_FL1);
  }
  asm volatile("s_waitcnt vmcnt(1)" ::: "memory");
  RAWBAR();
  if (b == 0 && t == 0) BST32(tag, 0, WS_EPOCH);
  VAALL(0)
  u32x4 cp[8];
  const char* cbase = ws + WS_CSP + (size_t)cbk * (8 * 224 * 16);
  const unsigned co = (t < 224 ? t : 0) * 16;
  u32x4 sx[2];
  {
    const char* sbase = ws + WS_X1P + (size_t)(rbk * 128 + cbk * 8) * 4;
    const unsigned so = (unsigned)(t >> 1) * (S * 4) + (t & 1) * 16;
    asm volatile("global_load_dwordx4 %0, %1, %2 sc1" : "=v"(sx[0]) : "v"(so), "s"(sbase) : "memory");
    asm volatile("global_load_dwordx4 %0, %1, %2 sc1" : "=v"(sx[1]) : "v"(so), "s"(sbase + (size_t)128 * (S * 4)) : "memory");
  }
  VAALL(1)

  f32x4 acc[14][2];
#pragma unroll
  for (int m = 0; m < 14; ++m) acc[m][0] = acc[m][1] = (f32x4){0.f, 0.f, 0.f, 0.f};
  const char* lsrc = sm + lane * 16;
  const char* lsa[2];
#pragma unroll
  for (int tau = 0; tau < 2; ++tau) lsa[tau] = sm + L_A + (4 * g * 128 + ((32 * w + 16 * tau + l15) ^ (16 * (g & 1)))) * 4;
  bf16x8 wf[2][7];
  u32x4 af[2][2];

#define WAITG(n)                                                              \
  SCHED_FENCE();                                                              \
  asm volatile("s_waitcnt vmcnt(" #n ") lgkmcnt(0)" ::: "memory");            \
  __builtin_amdgcn_s_barrier();                                               \
  asm volatile("" ::: "memory");                                              \
  SCHED_FENCE();
  unsigned lb2[2] = {lbase + lane * 16, lbase + 2 * G_W + lane * 16};
  asm volatile("" : "+v"(lb2[0]), "+v"(lb2[1]));
  typedef __attribute__((address_space(3))) bf16x8* lds_f8p;
#define WFR(i, m) (*(lds_f8p)(uintptr_t)(lb2[((i) >> 3) & 1] + (((i) >> 2) & 1) * G_W + ((i) & 3) * 7168 + (m) * 1024))
#define AOFF(ks) ((((ks) >> 1) % 3) * G_A + ((ks) & 1) * 8192)
#define RD(i, m) wf[(i) & 1][m] = WFR(i, m);
#define RA(dst, ks, tau)                                                             \
  {                                                                                  \
    dst[0] = *(const u32*)(lsa[tau] + AOFF(ks));                                     \
    dst[1] = *(const u32*)(lsa[tau] + AOFF(ks) + 512);                               \
    dst[2] = *(const u32*)(lsa[tau] + AOFF(ks) + 1024);                              \
    dst[3] = *(const u32*)(lsa[tau] + AOFF(ks) + 1536);                              \
  }
#define MF(T, tau, A_, B_) acc[T][tau] = __builtin_amdgcn_mfma_f32_16x16x32_bf16(A_, __builtin_bit_cast(bf16x8, B_), acc[T][tau], 0, 0, 0);
#define M1(i, m, tau) MF(7 * ((i) & 1) + (m), tau, wf[(i) & 1][m], af[((i) >> 1) & 1][tau])
#define VX(ga, gw, q0, j)                                                                        \
  {                                                                                              \
    if ((ga) >= 0 && (j) < 4) VA((ga) < 0 ? 0 : (ga), (j) & 3)                                   \
    if ((gw) >= 0 && (j) >= 4 && (j) < 11) VW((gw) < 0 ? 0 : (gw), (j) - 4)                      \
    if ((q0) >= 0 && (j) >= 4 && (j) < 11) WDM((q0) + (j) - 4)                                   \
  }
#define RAN(i, tau) { if ((i) & 1) RA(af[(((i) + 1) >> 1) & 1][tau], ((i) + 1) >> 1, tau) }
#define STEP(i, ga, gw, q0, j0)                                                        \
  {                                                                                    \
    M1(i, 0, 0) RD((i) + 1, 0) SCHED_FENCE(); M1(i, 0, 1) RAN(i, 0) SCHED_FENCE();     \
    M1(i, 1, 0) RD((i) + 1, 1) SCHED_FENCE(); M1(i, 1, 1) RAN(i, 1) SCHED_FENCE();     \
    M1(i, 2, 0) RD((i) + 1, 2) SCHED_FENCE(); M1(i, 2, 1) RD((i) + 1, 3) SCHED_FENCE(); \
    M1(i, 3, 0) RD((i) + 1, 4) SCHED_FENCE(); M1(i, 3, 1) RD((i) + 1, 5) SCHED_FENCE(); \
    M1(i, 4, 0) RD((i) + 1, 6) SCHED_FENCE(); M1(i, 4, 1) VX(ga, gw, q0, j0) SCHED_FENCE(); \
    M1(i, 5, 0) SCHED_FENCE(); M1(i, 5, 1) VX(ga, gw, q0, (j0) + 1) SCHED_FENCE();     \
    M1(i, 6, 0) SCHED_FENCE(); M1(i, 6, 1) VX(ga, gw, q0, (j0) + 2) SCHED_FENCE();     \
  }
#define PIN_ACC()                                                                                                  \
  _Pragma("unroll") for (int m_ = 0; m_ < 14; m_ += 2)                                                             \
      asm volatile("" : "+a"(acc[m_][0]), "+a"(acc[m_][1]), "+a"(acc[m_ + 1][0]), "+a"(acc[m_ + 1][1]));
#define STEP4(i, ga, gw, q0) STEP(i, ga, gw, q0, 0) STEP((i) + 1, ga, gw, q0, 3) STEP((i) + 2, ga, gw, q0, 6) STEP((i) + 3, ga, gw, q0, 9)
  SCHED_FENCE();
  asm volatile("s_waitcnt vmcnt(4)" : "+v"(sx[0]), "+v"(sx[1])::"memory");
  {
    float sv[8];
#pragma unroll
    for (int e = 0; e < 4; ++e) {
      sv[e] = red_2_64(dot2(sx[1][e], 0x3f803f80u, dot2(sx[0][e], 0x3f803f80u, 0.f)));
      sv[4 + e] = red_2_64(dot2(sx[1][e], sx[1][e], dot2(sx[0][e], sx[0][e], 0.f)));
    }
    if (lane < 2) {
      float* ps = (float*)(sm + L_A + 2 * G_A) + (w * 2 + lane) * 8;
      *(f32x4*)ps = (f32x4){sv[0], sv[1], sv[2], sv[3]};
      *(f32x4*)(ps + 4) = (f32x4){sv[4], sv[5], sv[6], sv[7]};
    }
  }
  WAITG(4)
  RA(af[0][0], 0, 0) RA(af[0][1], 0, 1)
  RD(0, 0) RD(0, 1) RD(0, 2) RD(0, 3) RD(0, 4) RD(0, 5) RD(0, 6)
  if (w == 0) {
    const float* ps = (const float*)(sm + L_A + 2 * G_A) + ((lane >> 2) & 1) * 8 + (lane & 3);
    const float s1 = (ps[0] + ps[16]) + (ps[32] + ps[48]);
    const float s2 = (ps[4] + ps[20]) + (ps[36] + ps[52]);
    const float mean = s1 * (1.f / D);
    const float var = fmaxf(s2 * (1.f / D) - mean * mean, 0.f);
    const float sdv = __builtin_amdgcn_sqrtf(rb(rb(var) + 1e-5f));
    u32x2 gr = {pk2(mean, sdv), tag};
    if (lane < 8) BST64(gr, (rbk * 128 + cbk * 8 + lane) * 8, WS_ST2G);
    asm volatile("s_waitcnt lgkmcnt(0)" ::: "memory");
  }
  SCHED_FENCE();
  VX(2, 3, -1, 0) VX(2, 3, -1, 1)
  STEP(0, 2, 3, -1, 2) STEP(1, 2, 3, -1, 5) STEP(2, 2, 3, -1, 8)
  WAITG(11)
  STEP4(3, 3, 4, -1)
  WAITG(18)
  STEP4(7, 4, 5, -1)
  WAITG(18)
  STEP4(11, 5, 6, -1)
  WAITG(18)
  STEP4(15, 6, 7, -1)
  u32x2 stg[2];
  const char* stp = ws + WS_ST2G + (size_t)st * 256;
  const unsigned sto = l15 * 8;
  asm volatile("global_load_dwordx2 %0, %1, %2 sc1" : "=v"(stg[0]) : "v"(sto), "s"(stp) : "memory");
  asm volatile("global_load_dwordx2 %0, %1, %2 offset:128 sc1" : "=v"(stg[1]) : "v"(sto), "s"(stp) : "memory");
#pragma unroll
  for (int i = 0; i < 8; ++i)
    asm volatile("global_load_dwordx4 %0, %1, %2 sc1" : "=v"(cp[i]) : "v"(co), "s"(cbase + i * 3584) : "memory");
  WAITG(28)
  STEP4(19, 7, -1, 0)
  WAITG(11)
  PIN_ACC()
  asm volatile("" : "+v"(cp[0]), "+v"(cp[1]), "+v"(cp[2]), "+v"(cp[3]), "+v"(cp[4]), "+v"(cp[5]), "+v"(cp[6]), "+v"(cp[7]));
  float cs = 0.f, bs = 0.f;
  u32 cbad = 0;
#pragma unroll
  for (int i = 0; i < 8; ++i) {
    cs += __uint_as_float(cp[i][0]);
    bs += __uint_as_float(cp[i][2]);
    cbad |= (cp[i][1] ^ tag) | (cp[i][3] ^ tag);
  }
  if (__builtin_expect(!__all(cbad == 0), 0)) {
#pragma nounroll
    for (int spins = 0; spins < SPIN_CAP; ++spins) {
      cs = 0.f; bs = 0.f; cbad = 0;
#pragma unroll
      for (int i = 0; i < 8; ++i) {
        const u32x4 c_ = BLD128(co, WS_CSP + (cbk * 8 + i) * 3584);
        cs += __uint_as_float(c_[0]);
        bs += __uint_as_float(c_[2]);
        cbad |= (c_[1] ^ tag) | (c_[3] ^ tag);
      }
      if (__all(cbad == 0)) break;
      __builtin_amdgcn_s_sleep(1);
    }
  }
  {
    const float c0 = rb(cs), c1 = rb(cs - c0), c2 = rb(cs - c0 - c1);
    const float b0 = rb(bs), b1 = rb(bs - b0), b2 = rb(bs - b0 - b1);
    *(u32x4*)(sm + L_COR + t * 16) = t < 224 ? (u32x4){pk2(c0, c1), pk2(c2, b0), pk2(b1, b2), 0u} : (u32x4){0u, 0u, 0u, 0u};
  }
  SCHED_FENCE();
  STEP4(23, -1, -1, 7)
  WAITG(14)
  asm volatile("" : "+v"(stg[0]), "+v"(stg[1]));
  PIN_ACC()
#pragma nounroll
  for (int spins = 0; !__all(stg[0][1] == tag && stg[1][1] == tag) && spins < SPIN_CAP; ++spins) {
    __builtin_amdgcn_s_sleep(1);
    asm volatile("global_load_dwordx2 %0, %1, %2 sc1\n\ts_waitcnt vmcnt(0)" : "=&v"(stg[0]) : "v"(sto), "s"(stp) : "memory");
    asm volatile("global_load_dwordx2 %0, %1, %2 offset:128 sc1\n\ts_waitcnt vmcnt(0)" : "=&v"(stg[1]) : "v"(sto), "s"(stp) : "memory");
  }
  SCHED_FENCE();
  bf16x8 tl[2][6];
  u32x4 a15[2], cb4[2];
  float rs2[2];
  const char* lco = sm + L_COR + (g == 0 ? l15 * 16 : 224 * 16);
  const int cstr = g == 0 ? 256 : 0;
#define TW(T, ks) WFR(2 * (ks) + ((T) >= 7 ? 1 : 0), (T) % 7)
#define RDT(m, k_)                                                                                        \
  {                                                                                                       \
    if ((k_) < 4) tl[(m) & 1][k_] = TW(2 * (m) + ((k_) & 1), 14 + ((k_) >> 1));                           \
    else tl[(m) & 1][k_] = *(const bf16x8*)(lco + (2 * (m) + ((k_) & 1)) * cstr);                         \
  }
  {
    M1(27, 0, 0) RA(af[0][0], 14, 0) SCHED_FENCE(); M1(27, 0, 1) RA(af[0][1], 14, 1) SCHED_FENCE();
    M1(27, 1, 0) RDT(0, 0) SCHED_FENCE(); M1(27, 1, 1) RDT(0, 1) SCHED_FENCE();
    M1(27, 2, 0) RA(a15[0], 15, 0) SCHED_FENCE(); M1(27, 2, 1) RA(a15[1], 15, 1) SCHED_FENCE();
    M1(27, 3, 0) RDT(0, 2) SCHED_FENCE(); M1(27, 3, 1) RDT(0, 3) SCHED_FENCE();
    M1(27, 4, 0) RDT(0, 4) SCHED_FENCE(); M1(27, 4, 1) RDT(0, 5) SCHED_FENCE();
    M1(27, 5, 0) WDM(14) SCHED_FENCE(); M1(27, 5, 1) WDM(15) SCHED_FENCE();
    M1(27, 6, 0) WDM(16) SCHED_FENCE(); M1(27, 6, 1) SCHED_FENCE();
#pragma unroll
    for (int tau = 0; tau < 2; ++tau) {
      const float mb = lo2f(stg[tau][0]), sd = hi2f(stg[tau][0]);
      const float rs = __builtin_amdgcn_rcpf(sd);
      rs2[tau] = rs * rs;
      cb4[tau] = (u32x4){pk2(-mb, -mb), pk2(-mb, sd), pk2(sd, sd), 0u};
    }
    SCHED_FENCE();
  }
  const unsigned hoff = WS_HHF + ((unsigned)st * 112 + cbk * 7) * 1024;
  u32 ed[2][2];
#define EPJ(m, tau, j)                                                                          \
  {                                                                                             \
    float u0 = acc[2 * (m) + 1][tau][2 * (j)], u1 = acc[2 * (m) + 1][tau][2 * (j) + 1];         \
    asm volatile("" : "+v"(u0), "+v"(u1));                                                      \
    float e0 = relu(acc[2 * (m)][tau][2 * (j)]) * u0 * rs2[tau];                                \
    float e1 = relu(acc[2 * (m)][tau][2 * (j) + 1]) * u1 * rs2[tau];                            \
    ed[tau][j] = pk2(e0, e1);                                                                   \
  }
#define EST(m)                                                                                  \
  {                                                                                             \
    auto s0_ = __builtin_amdgcn_permlane16_swap(ed[0][0], ed[1][0], false, false);              \
    auto s1_ = __builtin_amdgcn_permlane16_swap(ed[0][1], ed[1][1], false, false);              \
    BST128(((u32x4){s0_[0], s1_[0], s0_[1], s1_[1]}), voff + (m) * 1024, hoff);                 \
  }
#define MT(m, k_, tau, B_) MF(2 * (m) + ((k_) & 1), tau, tl[(m) & 1][k_], B_)
#define PM(m) ((m) > 0 ? (m) - 1 : 0)
#define TAIL(m, X)                                                                                                    \
  {                                                                                                                   \
    MT(m, 0, 0, af[0][0]) if ((m) < 6) RDT((m) + 1, 0) SCHED_FENCE(); MT(m, 1, 0, af[0][0]) if ((m) < 6) RDT((m) + 1, 1) SCHED_FENCE(); \
    MT(m, 0, 1, af[0][1]) if ((m) < 6) RDT((m) + 1, 2) SCHED_FENCE(); MT(m, 1, 1, af[0][1]) if ((m) < 6) RDT((m) + 1, 3) SCHED_FENCE(); \
    MT(m, 2, 0, a15[0]) if ((m) > 0) EPJ(PM(m), 0, 0) SCHED_FENCE(); MT(m, 3, 0, a15[0]) if ((m) < 6) RDT((m) + 1, 4) SCHED_FENCE();    \
    MT(m, 2, 1, a15[1]) if ((m) > 0) EPJ(PM(m), 0, 1) SCHED_FENCE(); MT(m, 3, 1, a15[1]) if ((m) < 6) RDT((m) + 1, 5) SCHED_FENCE();    \
    MT(m, 4, 0, cb4[0]) if ((m) > 0) EPJ(PM(m), 1, 0) SCHED_FENCE(); MT(m, 5, 0, cb4[0]) X SCHED_FENCE();             \
    MT(m, 4, 1, cb4[1]) if ((m) > 0) EPJ(PM(m), 1, 1) SCHED_FENCE(); MT(m, 5, 1, cb4[1]) if ((m) > 0) EST(PM(m)) SCHED_FENCE(); \
  }
  TAIL(0, WDM(17) WDM(18) WDM(19)) TAIL(1, ) TAIL(2, ) TAIL(3, ) TAIL(4, ) TAIL(5, ) TAIL(6, )
  EPJ(6, 0, 0) EPJ(6, 0, 1) EPJ(6, 1, 0) EPJ(6, 1, 1) EST(6)
  asm volatile("s_waitcnt vmcnt(0)" ::: "memory");
  BST32(tag, ((st * 16 + cbk) * 64) | IF_(lane == 0), WS_FL2);
  SCHED_FENCE();

  const unsigned dso[2] = {WS_WDF + ((unsigned)(2 * nbk + (w >> 1)) * 112 + 28 * w) * 1024, WS_WDF + ((unsigned)(2 * nbk + 1 - (w >> 1)) * 112 + 28 * w) * 1024};
#pragma unroll
  for (int r = WD_EARLY; r < 36; ++r) wr2[r] = BLD128(voff, dso[r & 1] + (10 + (r >> 1)) * 1024);
  const int fn = w >> 1, fs = w & 1;
  const int ncol = 64 * nbk + 32 * fn;
  const int srow = 64 * sbk + 32 * fs + (lane & 31);
  u32 xr[8];
#pragma unroll
  for (int i = 0; i < 8; ++i)
    xr[i] = BLD32(((4 * h) * S + (lane & 31)) * 4, WS_X1P + (((ncol >> 1) + (i >> 2) * 8 + (i & 3)) * S + 64 * sbk + 32 * fs) * 4);
  SCHED_FENCE();
#pragma nounroll
  for (int spins = 0; spins < SPIN_CAP; ++spins) {
    const u32 f = BLD32(((2 * sbk + (lane & 1)) * 16 + 4 * w + ((lane >> 1) & 3)) * 64, WS_FL2);
    if (__all(f == tag)) break;
    __builtin_amdgcn_s_sleep(1);
  }
  __builtin_amdgcn_fence(__ATOMIC_ACQUIRE, "wavefront");
  SCHED_FENCE();
  const unsigned hs = WS_HHF + ((unsigned)(2 * sbk + fs) * 112 + 28 * w) * 1024;
  const unsigned hs1 = WS_HHF + ((unsigned)(2 * sbk + 1 - fs) * 112 + 28 * w) * 1024;
  constexpr int WIN = 16;
  u32x4 hb[WIN][2];
#define HLD(i)                                                  \
  {                                                             \
    hb[(i) % WIN][0] = BLD128(voff, hs + (i) * 1024);           \
    hb[(i) % WIN][1] = BLD128(voff, hs1 + (i) * 1024);         \
  }
#pragma unroll
  for (int i = 0; i < WIN; ++i) HLD(i)
  SCHED_FENCE();
  f32x16 ac2[2][2];
#pragma unroll
  for (int a = 0; a < 2; ++a)
#pragma unroll
    for (int c = 0; c < 2; ++c)
#pragma unroll
      for (int r = 0; r < 16; ++r) ac2[a][c][r] = 0.f;
  bf16x8 wl2[2][2];
  wl2[0][0] = *(const bf16x8*)(lsrc + wd_lds_off(0, w));
  wl2[0][1] = *(const bf16x8*)(lsrc + wd_lds_off(1, w));
#pragma unroll
  for (int i = 0; i < 28; ++i) {
    if (i + 1 < 10) {
      wl2[(i + 1) & 1][0] = *(const bf16x8*)(lsrc + wd_lds_off(2 * (i + 1), w));
      wl2[(i + 1) & 1][1] = *(const bf16x8*)(lsrc + wd_lds_off(2 * (i + 1) + 1, w));
    }
#pragma unroll
    for (int a = 0; a < 2; ++a)
#pragma unroll
      for (int c = 0; c < 2; ++c)
        ac2[a][c] = __builtin_amdgcn_mfma_f32_32x32x16_bf16(
            i < 10 ? wl2[i & 1][a] : __builtin_bit_cast(bf16x8, wr2[i < 10 ? 0 : 2 * (i - 10) + a]),
            __builtin_bit_cast(bf16x8, hb[i % WIN][c]), ac2[a][c], 0, 0, 0);
    if (i + WIN < 28) HLD(i + WIN)
    SCHED_FENCE();
  }
#pragma unroll
  for (int j = 1; j < 4; ++j) {
    char* dst = sm + (j - 1) * G_W + w * 7168 + lane * 16;
#pragma unroll
    for (int r = 0; r < 4; ++r)
      *(f32x4*)(dst + r * 1024) = (f32x4){ac2[j >> 1][j & 1][4 * r], ac2[j >> 1][j & 1][4 * r + 1],
                                          ac2[j >> 1][j & 1][4 * r + 2], ac2[j >> 1][j & 1][4 * r + 3]};
  }
  RAWBAR();
  f32x16 fin = ac2[0][0];
#pragma unroll
  for (int j = 1; j < 4; ++j) {
    const char* srcp = sm + (j - 1) * G_W + (w ^ j) * 7168 + lane * 16;
#pragma unroll
    for (int r = 0; r < 4; ++r) {
      f32x4 vv = *(const f32x4*)(srcp + r * 1024);
#pragma unroll
      for (int e = 0; e < 4; ++e) fin[4 * r + e] += vv[e];
    }
  }
  u32 pkd[8];
#pragma unroll
  for (int j = 0; j < 8; ++j) pkd[j] = pk2(fin[2 * j], fin[2 * j + 1]);
  u16* orow = out + (size_t)srow * D + ncol + 8 * h;
#pragma unroll
  for (int kk = 0; kk < 2; ++kk) {
    auto r0 = __builtin_amdgcn_permlane32_swap(pkd[4 * kk], pkd[4 * kk + 2], false, false);
    auto r1 = __builtin_amdgcn_permlane32_swap(pkd[4 * kk + 1], pkd[4 * kk + 3], false, false);
    u32 m4[4] = {r0[0], r1[0], r0[1], r1[1]};
    u32x4 o;
#pragma unroll
    for (int j = 0; j < 4; ++j) {
      u32 a = pk2(gbv[8 * kk + 2 * j] * lo2f(m4[j]), gbv[8 * kk + 2 * j + 1] * hi2f(m4[j]));
      o[j] = pk2(lo2f(xr[4 * kk + j]) + lo2f(a), hi2f(xr[4 * kk + j]) + hi2f(a));
    }
    *(u32x4*)(orow + 16 * kk) = o;
  }
}

__global__ void k_end(char* ws) {
  if (ws == nullptr) __builtin_trap();
}

extern "C" void kernel_launch(void* const* d_in, const int* in_sizes, int n_in, void* d_out, int out_size,
                              void* d_ws, size_t ws_size, hipStream_t stream) {
  (void)hipFuncSetAttribute((const void*)mega, hipFuncAttributeMaxDynamicSharedMemorySize, LDS_BYTES);
  mega<<<256, 256, LDS_BYTES, stream>>>((const u16*)d_in[0], (const u16*)d_in[1], (const u16*)d_in[2],
                                        (const u16*)d_in[3], (const u16*)d_in[4], (const u16*)d_in[5],
                                        (const u16*)d_in[7], (const u16*)d_in[8], (const u16*)d_in[9],
                                        (const u16*)d_in[10], (const u16*)d_in[11], (char*)d_ws, (u16*)d_out);
}
```
